```python
import torch

def _softmax(x, axis=-1):
    return torch.softmax(x, dim=axis)
def _gelu(x):
    return torch.nn.functional.gelu(x, approximate="tanh")
def _silu(x):
    return torch.nn.functional.silu(x)
def _sigmoid(x):
    return torch.sigmoid(x)
def _layernorm(x, axis=-1, eps=1e-5):
    m = x.mean(dim=axis, keepdim=True)
    v = x.var(dim=axis, keepdim=True, unbiased=False)
    return (x - m) / torch.sqrt(v + eps)
def _scatter_add(tgt, idx, src, axis=0):
    return tgt.clone().index_add_(axis, idx, src)
def _segment_sum(src, seg, n):
    out = torch.zeros((n,) + src.shape[1:], dtype=src.dtype)
    return out.index_add_(0, seg, src)
def _pad(x, pw):
    flat = tuple(v for pair in reversed(pw) for v in pair)
    return torch.nn.functional.pad(x, flat)

kernel_name = 'standalone__kv_cache_quant_int4_int2_kv_with_per_channel_or_per_token_scales__decode'

def setup_inputs():

    _w = (lambda a: (lambda t: t.to(torch.bfloat16) if t.is_floating_point() else t)(torch.as_tensor(a)))
    B = 2
    Sq = 1
    Skv = 32768
    Hq = 32
    Hkv = 4
    Dh = 128
    bits = 3
    G = 128
    if G <= 0 or G >= Skv:
        G = max(1, Skv // 2)
    Skv = (Skv // G) * G
    ngk = Skv // G
    qmax = (1 << bits) - 1
    return {
        "q": _w((lambda *s: torch.randn(*s, dtype=torch.bfloat16) * 1.6)(B, Hq, Sq, Dh)),
        "qk": _w(torch.randint(0, qmax + 1, (B, Hkv, Skv, Dh)).to(torch.int32)),
        "qv": _w(torch.randint(0, qmax + 1, (B, Hkv, Skv, Dh)).to(torch.int32)),
        "k_scales": _w(torch.abs((lambda *s: torch.randn(*s, dtype=torch.bfloat16) * 1.6)(B, Hkv, ngk, Dh)) * 0.05 + 0.02),
        "k_zeros": _w((lambda *s: torch.randn(*s, dtype=torch.bfloat16) * 1.6)(B, Hkv, ngk, Dh) + float(qmax) / 2.0),
        "v_scales": _w(torch.abs((lambda *s: torch.randn(*s, dtype=torch.bfloat16) * 0.05)(B, Hkv, Skv, 1)) * 0.05 + 0.02),
        "v_zeros": _w((lambda *s: torch.randn(*s, dtype=torch.bfloat16) * 0.05)(B, Hkv, Skv, 1) + float(qmax) / 2.0),
    }

def reference(q, qk, qv, k_scales, k_zeros, v_scales, v_zeros):
    B, Hq, Sq, Dh = q.shape
    Hkv, Skv = qk.shape[1], qk.shape[2]
    ngk = k_scales.shape[2]
    G = Skv // ngk
    ks = (lambda x, n, axis: torch.repeat_interleave(x, n, dim=axis))(k_scales, G, axis=2)
    kz = (lambda x, n, axis: torch.repeat_interleave(x, n, dim=axis))(k_zeros, G, axis=2)
    k = ((lambda x, dt: x.to(dt))(qk, q.dtype) - kz) * ks
    v = ((lambda x, dt: x.to(dt))(qv, q.dtype) - v_zeros) * v_scales
    rep = Hq // Hkv
    if rep > 1:
        k = (lambda x, n, axis: torch.repeat_interleave(x, n, dim=axis))(k, rep, axis=1)
        v = (lambda x, n, axis: torch.repeat_interleave(x, n, dim=axis))(v, rep, axis=1)
    scale = float(Dh) ** -0.5
    logits = torch.einsum("bhqd,bhkd->bhqk", q, k) * scale
    p = _softmax(logits, axis=-1)
    o = torch.einsum("bhqk,bhkd->bhqd", p, v)
    return o
```

```cpp
#include <hip/hip_runtime.h>
#include <hip/hip_bf16.h>
#include <stdint.h>

typedef unsigned u32;
typedef u32 u32x4 __attribute__((ext_vector_type(4)));
typedef u32 u32x2 __attribute__((ext_vector_type(2)));
typedef int i32x4 __attribute__((ext_vector_type(4)));
typedef float f32x4 __attribute__((ext_vector_type(4)));
typedef float f32x2 __attribute__((ext_vector_type(2)));
typedef __bf16 bf16x8 __attribute__((ext_vector_type(8)));
typedef __bf16 bf16x2 __attribute__((ext_vector_type(2)));

#define KV_NT 1
#define WIN 24

constexpr int B_ = 2, HQ = 32, HKV = 4, SKV = 32768, DH = 128, GT = 128, NGK = SKV / GT;
constexpr int REP = HQ / HKV;
constexpr int NBH = B_ * HKV;
constexpr int NW = 8;
constexpr int NSPLIT = NGK / NW;
constexpr float QSCALE = 0.088388347648318440f * 1.4426950408889634f;

__device__ __forceinline__ float bf_lo(u32 x) { return __builtin_bit_cast(float, x << 16); }
__device__ __forceinline__ float bf_hi(u32 x) { return __builtin_bit_cast(float, x & 0xffff0000u); }
__device__ __forceinline__ u32 pk_bf16(float a, float b) {
  bf16x2 r; r[0] = (__bf16)a; r[1] = (__bf16)b; return __builtin_bit_cast(u32, r);
}
__device__ __forceinline__ i32x4 ld_stream(const int* p) {
  return __builtin_nontemporal_load(reinterpret_cast<const i32x4*>(p));
}
template <int CTRL> __device__ __forceinline__ float dpp(float x) {
  return __builtin_bit_cast(float, __builtin_amdgcn_mov_dpp(__builtin_bit_cast(int, x), CTRL, 0xf, 0xf, true));
}
__device__ __forceinline__ float xrow_max(float x) {
  auto s = __builtin_amdgcn_permlane16_swap(__float_as_uint(x), __float_as_uint(x), false, false);
  x = fmaxf(__uint_as_float(s[0]), __uint_as_float(s[1]));
  auto t = __builtin_amdgcn_permlane32_swap(__float_as_uint(x), __float_as_uint(x), false, false);
  return fmaxf(__uint_as_float(t[0]), __uint_as_float(t[1]));
}
__device__ __forceinline__ float xrow_sum(float x) {
  auto s = __builtin_amdgcn_permlane16_swap(__float_as_uint(x), __float_as_uint(x), false, false);
  x = __uint_as_float(s[0]) + __uint_as_float(s[1]);
  auto t = __builtin_amdgcn_permlane32_swap(__float_as_uint(x), __float_as_uint(x), false, false);
  return __uint_as_float(t[0]) + __uint_as_float(t[1]);
}
__device__ __forceinline__ bf16x8 cvt8(const i32x4& a, const i32x4& b) {
  u32x4 r = {pk_bf16((float)a[0], (float)a[1]), pk_bf16((float)a[2], (float)a[3]),
             pk_bf16((float)b[0], (float)b[1]), pk_bf16((float)b[2], (float)b[3])};
  return __builtin_bit_cast(bf16x8, r);
}

constexpr size_t WS_PO = 0, WS_PML = (size_t)NBH * NSPLIT * REP * DH * 4;

__global__ void __launch_bounds__(NW * 64, 2)
kivi_split(const u32* __restrict__ q, const int* __restrict__ qk, const int* __restrict__ qv,
           const u32* __restrict__ ks, const u32* __restrict__ kz,
           const u32* __restrict__ vs, const u32* __restrict__ vz, float* __restrict__ ws) {
  __shared__ __attribute__((aligned(16))) float lds[NW * REP * DH + NW * REP * 4];
  const int tid = threadIdx.x, lane = tid & 63;
  const int wave = __builtin_amdgcn_readfirstlane(tid >> 6);
  const int c = lane & 15, g = lane >> 4;
  const int bh = blockIdx.x & 7, split = blockIdx.x >> 3;
  const int grp = split * NW + wave;
  const size_t tok0 = (size_t)bh * SKV + (size_t)grp * GT;

  u32x2 qr[8], ksr[8], kzr[8];
  {
    const u32* qp = q + ((size_t)bh * REP + (c & 7)) * (DH / 2) + 2 * g;
    const u32* sp = ks + ((size_t)bh * NGK + grp) * (DH / 2) + 2 * g;
    const u32* zp = kz + ((size_t)bh * NGK + grp) * (DH / 2) + 2 * g;
#pragma unroll
    for (int i = 0; i < 8; ++i) {
      qr[i] = *reinterpret_cast<const u32x2*>(qp + 8 * i);
      ksr[i] = *reinterpret_cast<const u32x2*>(sp + 8 * i);
      kzr[i] = *reinterpret_cast<const u32x2*>(zp + 8 * i);
    }
  }
  u32x4 vsr[4], vzr[4];
  {
    const u32* sp = vs + (tok0 + 32 * g) / 2;
    const u32* zp = vz + (tok0 + 32 * g) / 2;
#pragma unroll
    for (int i = 0; i < 4; ++i) {
      vsr[i] = *reinterpret_cast<const u32x4*>(sp + 4 * i);
      vzr[i] = *reinterpret_cast<const u32x4*>(zp + 4 * i);
    }
  }
  const int* kp = qk + (tok0 + 32 * (c >> 2) + (c & 3)) * DH + 4 * g;
  const int* vp = qv + (tok0 + 32 * g) * DH + 4 * c;
  i32x4 buf[128];
#define ISSUE(n_) do { constexpr int n = (n_); if (n < 64) buf[n] = ld_stream(kp + (n >> 3) * 4 * DH + (n & 7) * 16); \
    else if (n < 128) buf[n] = ld_stream(vp + (((n - 64) >> 4) * 8 + (n & 7)) * DH + (((n - 64) >> 3) & 1) * 64); } while (0)
#define ISSUE2(n_) do { ISSUE(n_); ISSUE((n_) + 1); } while (0)
#define ISSUE8(n_) do { ISSUE2(n_); ISSUE2((n_) + 2); ISSUE2((n_) + 4); ISSUE2((n_) + 6); } while (0)
  __builtin_amdgcn_sched_barrier(0);
  ISSUE8(0); ISSUE8(8);
  ISSUE8(16);
  __builtin_amdgcn_sched_barrier(0);

  bf16x8 qf[4];
  float cq = 0.f;
  {
    const bool lo = c >= 8;
    u32 w[16];
#pragma unroll
    for (int i = 0; i < 8; ++i)
#pragma unroll
      for (int t = 0; t < 2; ++t) {
        const float a0 = bf_lo(qr[i][t]) * bf_lo(ksr[i][t]) * QSCALE, a1 = bf_hi(qr[i][t]) * bf_hi(ksr[i][t]) * QSCALE;
        cq = fmaf(a0, bf_lo(kzr[i][t]), cq); cq = fmaf(a1, bf_hi(kzr[i][t]), cq);
        const u32 hi = pk_bf16(a0, a1);
        const u32 lw = pk_bf16(a0 - bf_lo(hi), a1 - bf_hi(hi));
        w[2 * i + t] = lo ? lw : hi;
      }
#pragma unroll
    for (int s = 0; s < 4; ++s) { u32x4 r = {w[4 * s], w[4 * s + 1], w[4 * s + 2], w[4 * s + 3]}; qf[s] = __builtin_bit_cast(bf16x8, r); }
    cq = xrow_sum(cq);
  }
  __builtin_amdgcn_sched_barrier(0);

  f32x4 S[8];
#define KSTEP(T, s) do { const bf16x8 kf = cvt8(buf[8 * (T) + 2 * (s)], buf[8 * (T) + 2 * (s) + 1]); \
    __builtin_amdgcn_sched_barrier(0); ISSUE2(WIN + 8 * (T) + 2 * (s)); __builtin_amdgcn_sched_barrier(0); \
    S[T] = __builtin_amdgcn_mfma_f32_16x16x32_bf16(kf, qf[s], (s) == 0 ? f32x4{0.f, 0.f, 0.f, 0.f} : S[T], 0, 0, 0); } while (0)
#define KTILE(T) do { KSTEP(T, 0); KSTEP(T, 1); KSTEP(T, 2); KSTEP(T, 3); } while (0)
  KTILE(0); KTILE(1); KTILE(2); KTILE(3); KTILE(4); KTILE(5); KTILE(6); KTILE(7);

  float m = -INFINITY;
#pragma unroll
  for (int T = 0; T < 8; ++T)
#pragma unroll
    for (int r = 0; r < 4; ++r) { const float s = S[T][r] + dpp<0x128>(S[T][r]) - cq; S[T][r] = s; m = fmaxf(m, s); }
  m = xrow_max(m);
  float l = 0.f, cv = 0.f;
  bf16x8 pf[4];
  {
    const bool lo = c >= 8;
    u32 w[16];
#pragma unroll
    for (int T = 0; T < 8; ++T)
#pragma unroll
      for (int t = 0; t < 2; ++t) {
        const float p0 = __builtin_amdgcn_exp2f(S[T][2 * t] - m), p1 = __builtin_amdgcn_exp2f(S[T][2 * t + 1] - m);
        l += p0 + p1;
        const u32 sv = vsr[T >> 1][(T & 1) * 2 + t], zv = vzr[T >> 1][(T & 1) * 2 + t];
        const float a0 = p0 * bf_lo(sv), a1 = p1 * bf_hi(sv);
        cv = fmaf(a0, bf_lo(zv), cv); cv = fmaf(a1, bf_hi(zv), cv);
        const u32 hi = pk_bf16(a0, a1);
        const u32 lw = pk_bf16(a0 - bf_lo(hi), a1 - bf_hi(hi));
        w[2 * T + t] = lo ? lw : hi;
      }
#pragma unroll
    for (int U = 0; U < 4; ++U) { u32x4 r = {w[4 * U], w[4 * U + 1], w[4 * U + 2], w[4 * U + 3]}; pf[U] = __builtin_bit_cast(bf16x8, r); }
    l = xrow_sum(l); cv = xrow_sum(cv);
  }
  float* mlc = lds + NW * REP * DH;
  if (lane < 8) { f32x4 r = {m, l, cv, 0.f}; *reinterpret_cast<f32x4*>(mlc + (wave * REP + lane) * 4) = r; }
  __builtin_amdgcn_sched_barrier(0);

  f32x4 acc[2][4];
#define VSTEP(U, h) do { constexpr int n0 = 64 + 16 * (U) + 8 * (h); bf16x8 vf[4]; \
    _Pragma("unroll") for (int e = 0; e < 4; ++e) { \
      u32x4 r = {pk_bf16((float)buf[n0][e], (float)buf[n0 + 1][e]), pk_bf16((float)buf[n0 + 2][e], (float)buf[n0 + 3][e]), \
                 pk_bf16((float)buf[n0 + 4][e], (float)buf[n0 + 5][e]), pk_bf16((float)buf[n0 + 6][e], (float)buf[n0 + 7][e])}; \
      vf[e] = __builtin_bit_cast(bf16x8, r); } \
    __builtin_amdgcn_sched_barrier(0); ISSUE8(WIN + n0); __builtin_amdgcn_sched_barrier(0); \
    _Pragma("unroll") for (int e = 0; e < 4; ++e) \
      acc[h][e] = __builtin_amdgcn_mfma_f32_16x16x32_bf16(pf[U], vf[e], (U) == 0 ? f32x4{0.f, 0.f, 0.f, 0.f} : acc[h][e], 0, 0, 0); } while (0)
  VSTEP(0, 0); VSTEP(0, 1); VSTEP(1, 0); VSTEP(1, 1); VSTEP(2, 0); VSTEP(2, 1); VSTEP(3, 0); VSTEP(3, 1);

#pragma unroll
  for (int r = 0; r < 4; ++r) {
    f32x4 o;
#pragma unroll
    for (int e = 0; e < 4; ++e) {
      auto s = __builtin_amdgcn_permlane32_swap(__float_as_uint(acc[0][e][r]), __float_as_uint(acc[1][e][r]), false, false);
      o[e] = __uint_as_float(s[0]) + __uint_as_float(s[1]);
    }
    *reinterpret_cast<f32x4*>(lds + (wave * REP + 4 * (g & 1) + r) * DH + 64 * (g >> 1) + 4 * c) = o;
  }
  __syncthreads();
  {
    const int h = wave;
    f32x4 rec[NW];
    float M = -INFINITY;
#pragma unroll
    for (int w = 0; w < NW; ++w) { rec[w] = *reinterpret_cast<const f32x4*>(mlc + (w * REP + h) * 4); M = fmaxf(M, rec[w][0]); }
    float L = 0.f, o0 = 0.f, o1 = 0.f;
#pragma unroll
    for (int w = 0; w < NW; ++w) {
      const float wt = __builtin_amdgcn_exp2f(rec[w][0] - M);
      const f32x2 a = *reinterpret_cast<const f32x2*>(lds + (w * REP + h) * DH + 2 * lane);
      L = fmaf(wt, rec[w][1], L); o0 = fmaf(wt, a[0] - rec[w][2], o0); o1 = fmaf(wt, a[1] - rec[w][2], o1);
    }
    const size_t slot = ((size_t)bh * NSPLIT + split) * REP + h;
    f32x2 o = {o0, o1};
    *reinterpret_cast<f32x2*>(ws + WS_PO / 4 + slot * DH + 2 * lane) = o;
    if (lane == 0) { f32x2 ml = {M, L}; *reinterpret_cast<f32x2*>(ws + WS_PML / 4 + slot * 2) = ml; }
  }
}

__global__ void __launch_bounds__(128) kivi_combine(const float* __restrict__ ws, __hip_bfloat16* __restrict__ out) {
  const int row = blockIdx.x;
  const int bh = row >> 3, h = row & 7, d = threadIdx.x;
  f32x2 ml[NSPLIT]; float o[NSPLIT];
#pragma unroll
  for (int s = 0; s < NSPLIT; ++s) {
    const size_t slot = ((size_t)bh * NSPLIT + s) * REP + h;
    ml[s] = *reinterpret_cast<const f32x2*>(ws + WS_PML / 4 + slot * 2);
    o[s] = ws[WS_PO / 4 + slot * DH + d];
  }
  float M = -INFINITY;
#pragma unroll
  for (int s = 0; s < NSPLIT; ++s) M = fmaxf(M, ml[s][0]);
  float L = 0.f, a = 0.f;
#pragma unroll
  for (int s = 0; s < NSPLIT; ++s) { const float wt = __builtin_amdgcn_exp2f(ml[s][0] - M); L = fmaf(wt, ml[s][1], L); a = fmaf(wt, o[s], a); }
  out[row * DH + d] = __float2bfloat16(a / L);
}

extern "C" void kernel_launch(void* const* d_in, const int* in_sizes, int n_in, void* d_out, int out_size,
                              void* d_ws, size_t ws_size, hipStream_t stream) {
  kivi_split<<<NBH * NSPLIT, NW * 64, 0, stream>>>((const u32*)d_in[0], (const int*)d_in[1], (const int*)d_in[2],
      (const u32*)d_in[3], (const u32*)d_in[4], (const u32*)d_in[5], (const u32*)d_in[6], (float*)d_ws);
  kivi_combine<<<NBH * REP, 128, 0, stream>>>((const float*)d_ws, (__hip_bfloat16*)d_out);
}
```

```cpp
#include <hip/hip_runtime.h>
#include <hip/hip_bf16.h>
#include <stdint.h>

typedef unsigned u32;
typedef u32 u32x4 __attribute__((ext_vector_type(4)));
typedef u32 u32x2 __attribute__((ext_vector_type(2)));
typedef float f32x4 __attribute__((ext_vector_type(4)));
typedef float f32x2 __attribute__((ext_vector_type(2)));
typedef __bf16 bf16x8 __attribute__((ext_vector_type(8)));
typedef __bf16 bf16x2 __attribute__((ext_vector_type(2)));
typedef short s16x4 __attribute__((ext_vector_type(4)));
typedef __amdgpu_buffer_rsrc_t rsrc_t;

constexpr int B_ = 2, HQ = 32, HKV = 4, SKV = 32768, DH = 128, GT = 128, NGK = SKV / GT;
constexpr int REP = HQ / HKV;
constexpr int NBH = B_ * HKV;
constexpr int NW = 4;
constexpr int NSPLIT = 32;
constexpr int TPW = SKV / NSPLIT / NW;
constexpr int NT = TPW / 16;
constexpr float QSCALE = 0.088388347648318440f * 1.4426950408889634f;

constexpr int KROW = 272, L_KT = 0, L_KP = 16 * KROW, L_VS = L_KP + 2048, L_VZ = L_VS + 1024, L_WAVE = L_VZ + 512;
constexpr int OROW = (DH + 4) * 4;
constexpr int L_MRG = NW * L_WAVE, L_ML = L_MRG + NW * REP * OROW, L_END = L_ML + NW * REP * 8;
constexpr int LDS_BYTES = 84 * 1024;
static_assert(L_END <= LDS_BYTES, "LDS map");

#define STAMP(i)

__device__ __forceinline__ float bf_lo(u32 x) { return __builtin_bit_cast(float, x << 16); }
__device__ __forceinline__ float bf_hi(u32 x) { return __builtin_bit_cast(float, x & 0xffff0000u); }
__device__ __forceinline__ u32 pk_bf16(float a, float b) {
  bf16x2 r; r[0] = (__bf16)a; r[1] = (__bf16)b; return __builtin_bit_cast(u32, r);
}
__device__ __forceinline__ float i2f(u32 x) { return (float)(int)x; }
template <int CTRL> __device__ __forceinline__ float dpp(float x) {
  return __builtin_bit_cast(float, __builtin_amdgcn_mov_dpp(__builtin_bit_cast(int, x), CTRL, 0xf, 0xf, true));
}
__device__ __forceinline__ float xrow_max(float x) {
  auto s = __builtin_amdgcn_permlane16_swap(__float_as_uint(x), __float_as_uint(x), false, false);
  x = fmaxf(__uint_as_float(s[0]), __uint_as_float(s[1]));
  auto t = __builtin_amdgcn_permlane32_swap(__float_as_uint(x), __float_as_uint(x), false, false);
  return fmaxf(__uint_as_float(t[0]), __uint_as_float(t[1]));
}
__device__ __forceinline__ float xrow_sum(float x) {
  auto s = __builtin_amdgcn_permlane16_swap(__float_as_uint(x), __float_as_uint(x), false, false);
  x = __uint_as_float(s[0]) + __uint_as_float(s[1]);
  auto t = __builtin_amdgcn_permlane32_swap(__float_as_uint(x), __float_as_uint(x), false, false);
  return __uint_as_float(t[0]) + __uint_as_float(t[1]);
}
#define SB() __builtin_amdgcn_sched_barrier(0)

constexpr size_t WS_PO = 0, WS_PML = (size_t)NBH * NSPLIT * REP * DH * 4;

__global__ void __launch_bounds__(NW * 64, 2)
kivi_split(const u32* __restrict__ q, const int* __restrict__ qk, const int* __restrict__ qv,
           const u32* __restrict__ ks, const u32* __restrict__ kz,
           const u32* __restrict__ vs, const u32* __restrict__ vz, float* __restrict__ ws) {
  extern __shared__ __attribute__((aligned(16))) unsigned char lds[];
  STAMP(0);
  const int tid = threadIdx.x, lane = tid & 63;
  const int wave = __builtin_amdgcn_readfirstlane(tid >> 6);
  const int c = lane & 15, g = lane >> 4;
  const int bh = blockIdx.x & 7, split = blockIdx.x >> 3;
  const int tw = (split * NW + wave) * TPW;
  const size_t tok0 = (size_t)bh * SKV + tw;
  const char* kb = (const char*)qk + tok0 * (DH * 4);
  const char* vb = (const char*)qv + tok0 * (DH * 4);
  const int voff = g * 2048 + c * 16;
  u32x4 K[8], V[8];
  {
    const rsrc_t rk = __builtin_amdgcn_make_buffer_rsrc((void*)kb, 0, TPW * DH * 4, 0x00020000);
    const rsrc_t rv = __builtin_amdgcn_make_buffer_rsrc((void*)vb, 0, TPW * DH * 4, 0x00020000);
#pragma unroll
    for (int n = 0; n < 8; ++n) K[n] = __builtin_amdgcn_raw_buffer_load_b128(rk, voff + (n >> 1) * 512 + (n & 1) * 256, 0, 2);
    SB();
    unsigned char* lw = lds + wave * L_WAVE;
    const size_t grow = ((size_t)bh * NGK + tw / GT) * (DH / 2);
    const u32 ks0 = ks[grow + lane], kz0 = kz[grow + lane], ks1 = ks[grow + 64 + lane], kz1 = kz[grow + 64 + lane];
    const u32x2 vsr = *reinterpret_cast<const u32x2*>(vs + tok0 / 2 + 2 * lane);
    const u32x2 vzr = *reinterpret_cast<const u32x2*>(vz + tok0 / 2 + 2 * lane);
    SB();
#pragma unroll
    for (int n = 0; n < 8; ++n) V[n] = __builtin_amdgcn_raw_buffer_load_b128(rv, voff + (n >> 1) * 512 + (n & 1) * 256, 0, 2);
    SB();
    *reinterpret_cast<f32x2*>(lw + L_KP + lane * 8) = f32x2{bf_lo(ks0), bf_hi(ks0)};
    *reinterpret_cast<f32x2*>(lw + L_KP + 512 + lane * 8) = f32x2{-bf_lo(kz0) * bf_lo(ks0), -bf_hi(kz0) * bf_hi(ks0)};
    *reinterpret_cast<f32x2*>(lw + L_KP + 1024 + lane * 8) = f32x2{bf_lo(ks1), bf_hi(ks1)};
    *reinterpret_cast<f32x2*>(lw + L_KP + 1536 + lane * 8) = f32x2{-bf_lo(kz1) * bf_lo(ks1), -bf_hi(kz1) * bf_hi(ks1)};
    *reinterpret_cast<f32x4*>(lw + L_VS + lane * 16) = f32x4{bf_lo(vsr[0]), bf_hi(vsr[0]), bf_lo(vsr[1]), bf_hi(vsr[1])};
    *reinterpret_cast<u32x2*>(lw + L_VZ + lane * 8) = u32x2{vzr[0] ^ 0x80008000u, vzr[1] ^ 0x80008000u};
  }
  bf16x8 qf[4];
  {
    const u32* qp = q + ((size_t)bh * REP + (c & 7)) * (DH / 2) + 4 * g;
#pragma unroll
    for (int s = 0; s < 4; ++s) qf[s] = __builtin_bit_cast(bf16x8, *reinterpret_cast<const u32x4*>(qp + 16 * s));
  }
  SB();

  unsigned char* lw = lds + wave * L_WAVE;
  unsigned char* kt_w = lw + L_KT + g * (4 * KROW) + c * 8;
  const unsigned char* kt_r = lw + L_KT + c * KROW + g * 16;
  const u32 lomask = c >= 8 ? 0xffff0000u : 0u;
  f32x4 acc[2][4], accz = {0.f, 0.f, 0.f, 0.f};
#pragma unroll
  for (int h = 0; h < 2; ++h)
#pragma unroll
    for (int e = 0; e < 4; ++e) acc[h][e] = f32x4{0.f, 0.f, 0.f, 0.f};
  float m = -INFINITY, l = 0.f;

#pragma unroll 1
  for (int it = 0; it < NT; ++it) {
    const bool more = it + 1 < NT;
    const int soff = more ? (it + 1) * 8192 : 0;
    const rsrc_t rk = __builtin_amdgcn_make_buffer_rsrc((void*)kb, 0, more ? TPW * DH * 4 : 0, 0x00020000);
    const rsrc_t rv = __builtin_amdgcn_make_buffer_rsrc((void*)vb, 0, more ? TPW * DH * 4 : 0, 0x00020000);
    const unsigned char* kp = lw + L_KP + (it >> 3) * 1024 + c * 16;
    f32x4 sc[2], zc[2];
#pragma unroll
    for (int h = 0; h < 2; ++h) { sc[h] = *reinterpret_cast<const f32x4*>(kp + h * 256); zc[h] = *reinterpret_cast<const f32x4*>(kp + 512 + h * 256); }
    const f32x4 vsc = *reinterpret_cast<const f32x4*>(lw + L_VS + it * 64 + g * 16);
    const u32x2 vzn = *reinterpret_cast<const u32x2*>(lw + L_VZ + it * 32 + g * 8);
    SB();
#pragma unroll
    for (int n = 0; n < 8; ++n) {
      const int h = n & 1;
      const u32x2 w = {pk_bf16(fmaf(i2f(K[n][0]), sc[h][0], zc[h][0]), fmaf(i2f(K[n][1]), sc[h][1], zc[h][1])),
                       pk_bf16(fmaf(i2f(K[n][2]), sc[h][2], zc[h][2]), fmaf(i2f(K[n][3]), sc[h][3], zc[h][3]))};
      *reinterpret_cast<u32x2*>(kt_w + (n >> 1) * KROW + h * 128) = w;
      SB();
      K[n] = __builtin_amdgcn_raw_buffer_load_b128(rk, voff + (n >> 1) * 512 + h * 256, soff, 2);
      SB();
    }
    bf16x8 kf[4];
#pragma unroll
    for (int s = 0; s < 4; ++s) kf[s] = __builtin_bit_cast(bf16x8, *reinterpret_cast<const u32x4*>(kt_r + 64 * s));
    SB();
    u32x2 vf[2][4];
#pragma unroll
    for (int mp = 0; mp < 2; ++mp)
#pragma unroll
      for (int h = 0; h < 2; ++h) {
        const int n0 = 4 * mp + h, n1 = n0 + 2;
#pragma unroll
        for (int e = 0; e < 4; ++e) vf[h][e][mp] = pk_bf16(i2f(V[n0][e]), i2f(V[n1][e]));
        SB();
        V[n0] = __builtin_amdgcn_raw_buffer_load_b128(rv, voff + (n0 >> 1) * 512 + h * 256, soff, 2);
        V[n1] = __builtin_amdgcn_raw_buffer_load_b128(rv, voff + (n1 >> 1) * 512 + h * 256, soff, 2);
        SB();
      }
    f32x4 S = {0.f, 0.f, 0.f, 0.f};
#pragma unroll
    for (int s = 0; s < 4; ++s) S = __builtin_amdgcn_mfma_f32_16x16x32_bf16(kf[s], qf[s], S, 0, 0, 0);
    const float mn = fmaxf(m, xrow_max(fmaxf(fmaxf(S[0], S[1]), fmaxf(S[2], S[3]))));
    const float alpha = __builtin_amdgcn_exp2f((m - mn) * QSCALE);
    const float mc = mn * QSCALE;
    m = mn;
    float p[4];
#pragma unroll
    for (int r = 0; r < 4; ++r) p[r] = __builtin_amdgcn_exp2f(fmaf(S[r], QSCALE, -mc));
    l = fmaf(l, alpha, (p[0] + p[1]) + (p[2] + p[3]));
    u32 y[4];
#pragma unroll
    for (int r = 0; r < 4; ++r) {
      const float x = p[r] * vsc[r];
      y[r] = __float_as_uint(x - __uint_as_float(__float_as_uint(x) & lomask));
    }
    const u32x2 pfu = {__builtin_amdgcn_perm(y[1], y[0], 0x07060302u), __builtin_amdgcn_perm(y[3], y[2], 0x07060302u)};
    const s16x4 pf = __builtin_bit_cast(s16x4, pfu);
    accz = __builtin_amdgcn_mfma_f32_16x16x16bf16_1k(__builtin_bit_cast(s16x4, vzn), pf, accz * alpha, 0, 0, 0);
#pragma unroll
    for (int h = 0; h < 2; ++h)
#pragma unroll
      for (int e = 0; e < 4; ++e)
        acc[h][e] = __builtin_amdgcn_mfma_f32_16x16x16bf16_1k(__builtin_bit_cast(s16x4, vf[h][e]), pf, acc[h][e] * alpha, 0, 0, 0);
    SB();
  }
  STAMP(1);

  l = xrow_sum(l);
  {
    const bool up = c >= 8;
    unsigned char* orow = lds + L_MRG + (wave * REP + (c & 7)) * OROW + (up ? 256 : 0) + g * 64;
#pragma unroll
    for (int r = 0; r < 4; ++r) {
      f32x4 o;
      const float z = accz[r] + dpp<0x128>(accz[r]);
#pragma unroll
      for (int e = 0; e < 4; ++e) {
        const float own = up ? acc[1][e][r] : acc[0][e][r], oth = up ? acc[0][e][r] : acc[1][e][r];
        o[e] = own + dpp<0x128>(oth) + z;
      }
      *reinterpret_cast<f32x4*>(orow + r * 16) = o;
    }
    if (lane < 8) *reinterpret_cast<f32x2*>(lds + L_ML + (wave * REP + lane) * 8) = f32x2{m * QSCALE, l};
  }
  __syncthreads();
  STAMP(2);
  {
    const int h = tid >> 5, d4 = tid & 31;
    f32x2 ml[NW];
    float M = -INFINITY;
#pragma unroll
    for (int w = 0; w < NW; ++w) { ml[w] = *reinterpret_cast<const f32x2*>(lds + L_ML + (w * REP + h) * 8); M = fmaxf(M, ml[w][0]); }
    float L = 0.f; f32x4 o = {0.f, 0.f, 0.f, 0.f};
#pragma unroll
    for (int w = 0; w < NW; ++w) {
      const float wt = __builtin_amdgcn_exp2f(ml[w][0] - M);
      const f32x4 a = *reinterpret_cast<const f32x4*>(lds + L_MRG + (w * REP + h) * OROW + d4 * 16);
      L = fmaf(wt, ml[w][1], L); o += a * wt;
    }
    const size_t slot = ((size_t)bh * NSPLIT + split) * REP + h;
    *reinterpret_cast<f32x4*>(ws + WS_PO / 4 + slot * DH + 4 * d4) = o;
    if (d4 == 0) *reinterpret_cast<f32x2*>(ws + WS_PML / 4 + slot * 2) = f32x2{M, L};
  }
}

__global__ void __launch_bounds__(128) kivi_combine(const float* __restrict__ ws, __hip_bfloat16* __restrict__ out) {
  const int row = blockIdx.x;
  const int bh = row & 7, h = row >> 3, d = threadIdx.x;
  f32x2 ml[NSPLIT]; float o[NSPLIT];
#pragma unroll
  for (int s = 0; s < NSPLIT; ++s) {
    const size_t slot = ((size_t)bh * NSPLIT + s) * REP + h;
    ml[s] = *reinterpret_cast<const f32x2*>(ws + WS_PML / 4 + slot * 2);
    o[s] = ws[WS_PO / 4 + slot * DH + d];
  }
  float M = -INFINITY;
#pragma unroll
  for (int s = 0; s < NSPLIT; ++s) M = fmaxf(M, ml[s][0]);
  float L = 0.f, a = 0.f;
#pragma unroll
  for (int s = 0; s < NSPLIT; ++s) { const float wt = __builtin_amdgcn_exp2f(ml[s][0] - M); L = fmaf(wt, ml[s][1], L); a = fmaf(wt, o[s], a); }
  out[(bh * REP + h) * DH + d] = __float2bfloat16(a / L);
}

extern "C" void kernel_launch(void* const* d_in, const int* in_sizes, int n_in, void* d_out, int out_size,
                              void* d_ws, size_t ws_size, hipStream_t stream) {
  (void)hipFuncSetAttribute((const void*)kivi_split, hipFuncAttributeMaxDynamicSharedMemorySize, LDS_BYTES);
  kivi_split<<<NBH * NSPLIT, NW * 64, LDS_BYTES, stream>>>((const u32*)d_in[0], (const int*)d_in[1], (const int*)d_in[2],
      (const u32*)d_in[3], (const u32*)d_in[4], (const u32*)d_in[5], (const u32*)d_in[6], (float*)d_ws);
  kivi_combine<<<NBH * REP, 128, 0, stream>>>((const float*)d_ws, (__hip_bfloat16*)d_out);
}
```

```cpp
#include <hip/hip_runtime.h>
#include <hip/hip_bf16.h>
#include <stdint.h>

typedef unsigned u32;
typedef u32 u32x4 __attribute__((ext_vector_type(4)));
typedef u32 u32x2 __attribute__((ext_vector_type(2)));
typedef float f32x4 __attribute__((ext_vector_type(4)));
typedef float f32x2 __attribute__((ext_vector_type(2)));
typedef __bf16 bf16x8 __attribute__((ext_vector_type(8)));
typedef __bf16 bf16x2 __attribute__((ext_vector_type(2)));
typedef short s16x4 __attribute__((ext_vector_type(4)));
typedef __amdgpu_buffer_rsrc_t rsrc_t;

#define AUXK 2
#define AUXV 2
constexpr int B_ = 2, HQ = 32, HKV = 4, SKV = 32768, DH = 128, GT = 128, NGK = SKV / GT;
constexpr int REP = HQ / HKV;
constexpr int NBH = B_ * HKV;
constexpr int NW = 4;
constexpr int NSPLIT = 32;
constexpr int TPW = SKV / NSPLIT / NW;
constexpr int NT = TPW / 16;
constexpr float QSCALE = 0.088388347648318440f * 1.4426950408889634f;

constexpr int KROW = 272, L_KT = 0, L_KP = 16 * KROW, L_VS = L_KP + 2048, L_VZ = L_VS + 1024, L_WAVE = L_VZ + 512;
constexpr int OROW = (DH + 4) * 4;
constexpr int L_MRG = NW * L_WAVE, L_ML = L_MRG + NW * REP * OROW, L_END = L_ML + NW * REP * 8;
constexpr int LDS_BYTES = 84 * 1024;
static_assert(L_END <= LDS_BYTES, "LDS map");

#define STAMP(i)

__device__ __forceinline__ float bf_lo(u32 x) { return __builtin_bit_cast(float, x << 16); }
__device__ __forceinline__ float bf_hi(u32 x) { return __builtin_bit_cast(float, x & 0xffff0000u); }
__device__ __forceinline__ u32 pk_bf16(float a, float b) {
  bf16x2 r; r[0] = (__bf16)a; r[1] = (__bf16)b; return __builtin_bit_cast(u32, r);
}
__device__ __forceinline__ float i2f(u32 x) { return (float)(int)x; }
template <int CTRL> __device__ __forceinline__ float dpp(float x) {
  return __builtin_bit_cast(float, __builtin_amdgcn_mov_dpp(__builtin_bit_cast(int, x), CTRL, 0xf, 0xf, true));
}
__device__ __forceinline__ float xrow_max(float x) {
  auto s = __builtin_amdgcn_permlane16_swap(__float_as_uint(x), __float_as_uint(x), false, false);
  x = fmaxf(__uint_as_float(s[0]), __uint_as_float(s[1]));
  auto t = __builtin_amdgcn_permlane32_swap(__float_as_uint(x), __float_as_uint(x), false, false);
  return fmaxf(__uint_as_float(t[0]), __uint_as_float(t[1]));
}
__device__ __forceinline__ float xrow_sum(float x) {
  auto s = __builtin_amdgcn_permlane16_swap(__float_as_uint(x), __float_as_uint(x), false, false);
  x = __uint_as_float(s[0]) + __uint_as_float(s[1]);
  auto t = __builtin_amdgcn_permlane32_swap(__float_as_uint(x), __float_as_uint(x), false, false);
  return __uint_as_float(t[0]) + __uint_as_float(t[1]);
}
#define SB() __builtin_amdgcn_sched_barrier(0)

constexpr size_t WS_PO = 0, WS_PML = (size_t)NBH * NSPLIT * REP * DH * 4;

__global__ void __launch_bounds__(NW * 64, 2)
kivi_split(const u32* __restrict__ q, const int* __restrict__ qk, const int* __restrict__ qv,
           const u32* __restrict__ ks, const u32* __restrict__ kz,
           const u32* __restrict__ vs, const u32* __restrict__ vz, float* __restrict__ ws) {
  extern __shared__ __attribute__((aligned(16))) unsigned char lds[];
  STAMP(0);
  const int tid = threadIdx.x, lane = tid & 63;
  const int wave = __builtin_amdgcn_readfirstlane(tid >> 6);
  const int c = lane & 15, g = lane >> 4;
  const int bh = blockIdx.x & 7, split = blockIdx.x >> 3;
  const int tw = (split * NW + wave) * TPW;
  const size_t tok0 = (size_t)bh * SKV + tw;
  const char* kb = (const char*)qk + tok0 * (DH * 4);
  const char* vb = (const char*)qv + tok0 * (DH * 4);
  const int voff = g * 2048 + c * 16;
  u32x4 K[8], V[8];
  {
    const rsrc_t rk = __builtin_amdgcn_make_buffer_rsrc((void*)kb, 0, TPW * DH * 4, 0x00020000);
    const rsrc_t rv = __builtin_amdgcn_make_buffer_rsrc((void*)vb, 0, TPW * DH * 4, 0x00020000);
#pragma unroll
    for (int n = 0; n < 8; ++n) K[n] = __builtin_amdgcn_raw_buffer_load_b128(rk, voff + (n >> 1) * 512 + (n & 1) * 256, 0, AUXK);
    SB();
    unsigned char* lw = lds + wave * L_WAVE;
    const size_t grow = ((size_t)bh * NGK + tw / GT) * (DH / 2);
    const u32 ks0 = ks[grow + lane], kz0 = kz[grow + lane], ks1 = ks[grow + 64 + lane], kz1 = kz[grow + 64 + lane];
    const u32x2 vsr = *reinterpret_cast<const u32x2*>(vs + tok0 / 2 + 2 * lane);
    const u32x2 vzr = *reinterpret_cast<const u32x2*>(vz + tok0 / 2 + 2 * lane);
    SB();
#pragma unroll
    for (int n = 0; n < 8; ++n) V[n] = __builtin_amdgcn_raw_buffer_load_b128(rv, voff + (n >> 1) * 512 + (n & 1) * 256, 0, AUXV);
    SB();
    *reinterpret_cast<f32x2*>(lw + L_KP + lane * 8) = f32x2{bf_lo(ks0), bf_hi(ks0)};
    *reinterpret_cast<f32x2*>(lw + L_KP + 512 + lane * 8) = f32x2{-bf_lo(kz0) * bf_lo(ks0), -bf_hi(kz0) * bf_hi(ks0)};
    *reinterpret_cast<f32x2*>(lw + L_KP + 1024 + lane * 8) = f32x2{bf_lo(ks1), bf_hi(ks1)};
    *reinterpret_cast<f32x2*>(lw + L_KP + 1536 + lane * 8) = f32x2{-bf_lo(kz1) * bf_lo(ks1), -bf_hi(kz1) * bf_hi(ks1)};
    *reinterpret_cast<f32x4*>(lw + L_VS + lane * 16) = f32x4{bf_lo(vsr[0]), bf_hi(vsr[0]), bf_lo(vsr[1]), bf_hi(vsr[1])};
    *reinterpret_cast<u32x2*>(lw + L_VZ + lane * 8) = u32x2{vzr[0] ^ 0x80008000u, vzr[1] ^ 0x80008000u};
  }
  bf16x8 qf[4];
  {
    const u32* qp = q + ((size_t)bh * REP + (c & 7)) * (DH / 2) + 4 * g;
#pragma unroll
    for (int s = 0; s < 4; ++s) qf[s] = __builtin_bit_cast(bf16x8, *reinterpret_cast<const u32x4*>(qp + 16 * s));
  }
  SB();

  unsigned char* lw = lds + wave * L_WAVE;
  unsigned char* kt_w = lw + L_KT + g * (4 * KROW) + c * 8;
  const unsigned char* kt_r = lw + L_KT + c * KROW + g * 16;
  const u32 lomask = c >= 8 ? 0xffff0000u : 0u;
  f32x4 acc[2][4], accz = {0.f, 0.f, 0.f, 0.f};
#pragma unroll
  for (int h = 0; h < 2; ++h)
#pragma unroll
    for (int e = 0; e < 4; ++e) acc[h][e] = f32x4{0.f, 0.f, 0.f, 0.f};
  float m = -INFINITY, l = 0.f;

#pragma unroll 1
  for (int it = 0; it < NT; ++it) {
    const bool more = it + 1 < NT;
    const int soff = more ? (it + 1) * 8192 : 0;
    const rsrc_t rk = __builtin_amdgcn_make_buffer_rsrc((void*)kb, 0, more ? TPW * DH * 4 : 0, 0x00020000);
    const rsrc_t rv = __builtin_amdgcn_make_buffer_rsrc((void*)vb, 0, more ? TPW * DH * 4 : 0, 0x00020000);
    const unsigned char* kp = lw + L_KP + (it >> 3) * 1024 + c * 16;
    f32x4 sc[2], zc[2];
#pragma unroll
    for (int h = 0; h < 2; ++h) { sc[h] = *reinterpret_cast<const f32x4*>(kp + h * 256); zc[h] = *reinterpret_cast<const f32x4*>(kp + 512 + h * 256); }
    const f32x4 vsc = *reinterpret_cast<const f32x4*>(lw + L_VS + it * 64 + g * 16);
    const u32x2 vzn = *reinterpret_cast<const u32x2*>(lw + L_VZ + it * 32 + g * 8);
    SB();
#pragma unroll
    for (int n = 0; n < 8; ++n) {
      const int h = n & 1;
      const u32x2 w = {pk_bf16(fmaf(i2f(K[n][0]), sc[h][0], zc[h][0]), fmaf(i2f(K[n][1]), sc[h][1], zc[h][1])),
                       pk_bf16(fmaf(i2f(K[n][2]), sc[h][2], zc[h][2]), fmaf(i2f(K[n][3]), sc[h][3], zc[h][3]))};
      *reinterpret_cast<u32x2*>(kt_w + (n >> 1) * KROW + h * 128) = w;
      SB();
      K[n] = __builtin_amdgcn_raw_buffer_load_b128(rk, voff + (n >> 1) * 512 + h * 256, soff, AUXK);
      SB();
    }
    bf16x8 kf[4];
#pragma unroll
    for (int s = 0; s < 4; ++s) kf[s] = __builtin_bit_cast(bf16x8, *reinterpret_cast<const u32x4*>(kt_r + 64 * s));
    SB();
    u32x2 vf[2][4];
#pragma unroll
    for (int mp = 0; mp < 2; ++mp)
#pragma unroll
      for (int h = 0; h < 2; ++h) {
        const int n0 = 4 * mp + h, n1 = n0 + 2;
#pragma unroll
        for (int e = 0; e < 4; ++e) vf[h][e][mp] = pk_bf16(i2f(V[n0][e]), i2f(V[n1][e]));
        SB();
        V[n0] = __builtin_amdgcn_raw_buffer_load_b128(rv, voff + (n0 >> 1) * 512 + h * 256, soff, AUXV);
        V[n1] = __builtin_amdgcn_raw_buffer_load_b128(rv, voff + (n1 >> 1) * 512 + h * 256, soff, AUXV);
        SB();
      }
    f32x4 S = {0.f, 0.f, 0.f, 0.f};
#pragma unroll
    for (int s = 0; s < 4; ++s) S = __builtin_amdgcn_mfma_f32_16x16x32_bf16(kf[s], qf[s], S, 0, 0, 0);
    const float mn = fmaxf(m, xrow_max(fmaxf(fmaxf(S[0], S[1]), fmaxf(S[2], S[3]))));
    const float alpha = __builtin_amdgcn_exp2f((m - mn) * QSCALE);
    const float mc = mn * QSCALE;
    m = mn;
    float p[4];
#pragma unroll
    for (int r = 0; r < 4; ++r) p[r] = __builtin_amdgcn_exp2f(fmaf(S[r], QSCALE, -mc));
    l = fmaf(l, alpha, (p[0] + p[1]) + (p[2] + p[3]));
    u32 y[4];
#pragma unroll
    for (int r = 0; r < 4; ++r) {
      const float x = p[r] * vsc[r];
      y[r] = __float_as_uint(x - __uint_as_float(__float_as_uint(x) & lomask));
    }
    const u32x2 pfu = {__builtin_amdgcn_perm(y[1], y[0], 0x07060302u), __builtin_amdgcn_perm(y[3], y[2], 0x07060302u)};
    const s16x4 pf = __builtin_bit_cast(s16x4, pfu);
    accz = __builtin_amdgcn_mfma_f32_16x16x16bf16_1k(__builtin_bit_cast(s16x4, vzn), pf, accz * alpha, 0, 0, 0);
#pragma unroll
    for (int h = 0; h < 2; ++h)
#pragma unroll
      for (int e = 0; e < 4; ++e)
        acc[h][e] = __builtin_amdgcn_mfma_f32_16x16x16bf16_1k(__builtin_bit_cast(s16x4, vf[h][e]), pf, acc[h][e] * alpha, 0, 0, 0);
    SB();
  }
  STAMP(1);

  l = xrow_sum(l);
  {
    const bool up = c >= 8;
    unsigned char* orow = lds + L_MRG + (wave * REP + (c & 7)) * OROW + (up ? 256 : 0) + g * 64;
#pragma unroll
    for (int r = 0; r < 4; ++r) {
      f32x4 o;
      const float z = accz[r] + dpp<0x128>(accz[r]);
#pragma unroll
      for (int e = 0; e < 4; ++e) {
        const float own = up ? acc[1][e][r] : acc[0][e][r], oth = up ? acc[0][e][r] : acc[1][e][r];
        o[e] = own + dpp<0x128>(oth) + z;
      }
      *reinterpret_cast<f32x4*>(orow + r * 16) = o;
    }
    if (lane < 8) *reinterpret_cast<f32x2*>(lds + L_ML + (wave * REP + lane) * 8) = f32x2{m * QSCALE, l};
  }
  __syncthreads();
  STAMP(2);
  {
    const int h = tid >> 5, d4 = tid & 31;
    f32x2 ml[NW];
    float M = -INFINITY;
#pragma unroll
    for (int w = 0; w < NW; ++w) { ml[w] = *reinterpret_cast<const f32x2*>(lds + L_ML + (w * REP + h) * 8); M = fmaxf(M, ml[w][0]); }
    float L = 0.f; f32x4 o = {0.f, 0.f, 0.f, 0.f};
#pragma unroll
    for (int w = 0; w < NW; ++w) {
      const float wt = __builtin_amdgcn_exp2f(ml[w][0] - M);
      const f32x4 a = *reinterpret_cast<const f32x4*>(lds + L_MRG + (w * REP + h) * OROW + d4 * 16);
      L = fmaf(wt, ml[w][1], L); o += a * wt;
    }
    const size_t slot = ((size_t)bh * NSPLIT + split) * REP + h;
    *reinterpret_cast<f32x4*>(ws + WS_PO / 4 + slot * DH + 4 * d4) = o;
    if (d4 == 0) *reinterpret_cast<f32x2*>(ws + WS_PML / 4 + slot * 2) = f32x2{M, L};
  }
}

__global__ void __launch_bounds__(128) kivi_combine(const float* __restrict__ ws, __hip_bfloat16* __restrict__ out) {
  const int row = blockIdx.x;
  const int bh = row & 7, h = row >> 3, d = threadIdx.x;
  f32x2 ml[NSPLIT]; float o[NSPLIT];
#pragma unroll
  for (int s = 0; s < NSPLIT; ++s) {
    const size_t slot = ((size_t)bh * NSPLIT + s) * REP + h;
    ml[s] = *reinterpret_cast<const f32x2*>(ws + WS_PML / 4 + slot * 2);
    o[s] = ws[WS_PO / 4 + slot * DH + d];
  }
  float M = -INFINITY;
#pragma unroll
  for (int s = 0; s < NSPLIT; ++s) M = fmaxf(M, ml[s][0]);
  float L = 0.f, a = 0.f;
#pragma unroll
  for (int s = 0; s < NSPLIT; ++s) { const float wt = __builtin_amdgcn_exp2f(ml[s][0] - M); L = fmaf(wt, ml[s][1], L); a = fmaf(wt, o[s], a); }
  out[(bh * REP + h) * DH + d] = __float2bfloat16(a / L);
}

extern "C" void kernel_launch(void* const* d_in, const int* in_sizes, int n_in, void* d_out, int out_size,
                              void* d_ws, size_t ws_size, hipStream_t stream) {
  (void)hipFuncSetAttribute((const void*)kivi_split, hipFuncAttributeMaxDynamicSharedMemorySize, LDS_BYTES);
  kivi_split<<<NBH * NSPLIT, NW * 64, LDS_BYTES, stream>>>((const u32*)d_in[0], (const int*)d_in[1], (const int*)d_in[2],
      (const u32*)d_in[3], (const u32*)d_in[4], (const u32*)d_in[5], (const u32*)d_in[6], (float*)d_ws);
  kivi_combine<<<NBH * REP, 128, 0, stream>>>((const float*)d_ws, (__hip_bfloat16*)d_out);
}
```

```cpp
#include <hip/hip_runtime.h>
#include <hip/hip_bf16.h>
#include <stdint.h>
#define PT_IN 1
#define EARLY 1

typedef unsigned u32;
typedef u32 u32x4 __attribute__((ext_vector_type(4)));
typedef u32 u32x2 __attribute__((ext_vector_type(2)));
typedef float f32x4 __attribute__((ext_vector_type(4)));
typedef float f32x2 __attribute__((ext_vector_type(2)));
typedef __bf16 bf16x8 __attribute__((ext_vector_type(8)));
typedef __bf16 bf16x2 __attribute__((ext_vector_type(2)));
typedef short s16x4 __attribute__((ext_vector_type(4)));
typedef __amdgpu_buffer_rsrc_t rsrc_t;

#define AUXK 2
#define AUXV 2
constexpr int B_ = 2, HQ = 32, HKV = 4, SKV = 32768, DH = 128, GT = 128, NGK = SKV / GT;
constexpr int REP = HQ / HKV;
constexpr int NBH = B_ * HKV;
constexpr int NW = 4;
constexpr int NSPLIT = 32;
constexpr int TPW = SKV / NSPLIT / NW;
constexpr int NT = TPW / 16;
constexpr float QSCALE = 0.088388347648318440f * 1.4426950408889634f;

constexpr int KROW = 272, L_KT = 0, L_KP = 16 * KROW, L_VS = L_KP + 2048, L_VZ = L_VS + 1024, L_WAVE = L_VZ + 512;
constexpr int OROW = (DH + 4) * 4;
constexpr int L_MRG = NW * L_WAVE, L_ML = L_MRG + NW * REP * OROW, L_END = L_ML + NW * REP * 8;
constexpr int LDS_BYTES = 84 * 1024;
static_assert(L_END <= LDS_BYTES, "LDS map");

#define STAMP(i)

__device__ __forceinline__ float bf_lo(u32 x) { return __builtin_bit_cast(float, x << 16); }
__device__ __forceinline__ float bf_hi(u32 x) { return __builtin_bit_cast(float, x & 0xffff0000u); }
__device__ __forceinline__ u32 pk_bf16(float a, float b) {
  bf16x2 r; r[0] = (__bf16)a; r[1] = (__bf16)b; return __builtin_bit_cast(u32, r);
}
__device__ __forceinline__ float i2f(u32 x) { return (float)(int)x; }
template <int CTRL> __device__ __forceinline__ float dpp(float x) {
  return __builtin_bit_cast(float, __builtin_amdgcn_mov_dpp(__builtin_bit_cast(int, x), CTRL, 0xf, 0xf, true));
}
__device__ __forceinline__ float xrow_max(float x) {
  auto s = __builtin_amdgcn_permlane16_swap(__float_as_uint(x), __float_as_uint(x), false, false);
  x = fmaxf(__uint_as_float(s[0]), __uint_as_float(s[1]));
  auto t = __builtin_amdgcn_permlane32_swap(__float_as_uint(x), __float_as_uint(x), false, false);
  return fmaxf(__uint_as_float(t[0]), __uint_as_float(t[1]));
}
__device__ __forceinline__ float xrow_sum(float x) {
  auto s = __builtin_amdgcn_permlane16_swap(__float_as_uint(x), __float_as_uint(x), false, false);
  x = __uint_as_float(s[0]) + __uint_as_float(s[1]);
  auto t = __builtin_amdgcn_permlane32_swap(__float_as_uint(x), __float_as_uint(x), false, false);
  return __uint_as_float(t[0]) + __uint_as_float(t[1]);
}
#define SB() __builtin_amdgcn_sched_barrier(0)

constexpr int PKT = 384, PKT_ALL = NSPLIT * NSPLIT * PKT, WS_GRP = 512 * 1024, WS_EPOCH = PKT_ALL;
constexpr int OOB = 0x7fffffe0;
constexpr int AUX_SC1 = 16;
#define ROT 3
#define SMALL_FIRST 1
#define SLEEP_FAR 16
#define SLEEP_NEAR 1

__global__ void __launch_bounds__(NW * 64, 2)
kivi_split(const u32* __restrict__ q, const int* __restrict__ qk, const int* __restrict__ qv,
           const u32* __restrict__ ks, const u32* __restrict__ kz,
           const u32* __restrict__ vs, const u32* __restrict__ vz, unsigned char* __restrict__ ws, u32* __restrict__ out) {
  extern __shared__ __attribute__((aligned(16))) unsigned char lds[];
  STAMP(0);
  const int tid = threadIdx.x, lane = tid & 63;
  const int wave = __builtin_amdgcn_readfirstlane(tid >> 6);
  const int c = lane & 15, g = lane >> 4;
  const int bh = blockIdx.x & 7, split = blockIdx.x >> 3;
  const int tw = (split * NW + wave) * TPW;
  const size_t tok0 = (size_t)bh * SKV + tw;
  const char* kb = (const char*)qk + tok0 * (DH * 4);
  const char* vb = (const char*)qv + tok0 * (DH * 4);
  const int voff = g * 2048 + c * 16;
  const int rot = ((blockIdx.x * NW + wave) * ROT) & (NT - 1);
  u32x4 K[8], V[8];
  bf16x8 qf[4];
  u32x2 epoch;
  u32 otouch;
  const rsrc_t rp = __builtin_amdgcn_make_buffer_rsrc((void*)(ws + (size_t)bh * WS_GRP), 0, WS_EPOCH + 8, 0x00020000);
  u32* outw = out + ((bh * REP + (split >> 2)) * DH + (split & 3) * 32) / 2;
  {
    const rsrc_t rk = __builtin_amdgcn_make_buffer_rsrc((void*)kb, 0, TPW * DH * 4, 0x00020000);
    const rsrc_t rv = __builtin_amdgcn_make_buffer_rsrc((void*)vb, 0, TPW * DH * 4, 0x00020000);
    unsigned char* lw = lds + wave * L_WAVE;
    const size_t grow = ((size_t)bh * NGK + tw / GT) * (DH / 2);
    const u32 ks0 = ks[grow + lane], kz0 = kz[grow + lane], ks1 = ks[grow + 64 + lane], kz1 = kz[grow + 64 + lane];
    const u32x2 vsr = *reinterpret_cast<const u32x2*>(vs + tok0 / 2 + 2 * lane);
    const u32x2 vzr = *reinterpret_cast<const u32x2*>(vz + tok0 / 2 + 2 * lane);
    SB();
#pragma unroll
    for (int n = 0; n < 8; ++n) K[n] = __builtin_amdgcn_raw_buffer_load_b128(rk, voff + (n >> 1) * 512 + (n & 1) * 256, rot * 8192, AUXK);
    SB();
#pragma unroll
    for (int i = 0; i < 8; ++i) { const int n = (i & 4) + ((i & 1) << 1) + ((i >> 1) & 1);
      V[n] = __builtin_amdgcn_raw_buffer_load_b128(rv, voff + (n >> 1) * 512 + (n & 1) * 256, rot * 8192, AUXV); }
    SB();
    const u32* qp = q + ((size_t)bh * REP + (c & 7)) * (DH / 2) + 4 * g;
#pragma unroll
    for (int s = 0; s < 4; ++s) qf[s] = __builtin_bit_cast(bf16x8, *reinterpret_cast<const u32x4*>(qp + 16 * s));
    epoch = __builtin_amdgcn_raw_buffer_load_b64(rp, 0, WS_EPOCH, AUX_SC1);
    otouch = __builtin_amdgcn_raw_buffer_load_b32(rp, (split * NSPLIT + 8 * wave + (lane >> 3)) * PKT + (lane & 3) * 128, 0, 16);
    otouch += outw[c];
    SB();
    *reinterpret_cast<f32x2*>(lw + L_KP + lane * 8) = f32x2{bf_lo(ks0), bf_hi(ks0)};
    *reinterpret_cast<f32x2*>(lw + L_KP + 512 + lane * 8) = f32x2{-bf_lo(kz0) * bf_lo(ks0), -bf_hi(kz0) * bf_hi(ks0)};
    *reinterpret_cast<f32x2*>(lw + L_KP + 1024 + lane * 8) = f32x2{bf_lo(ks1), bf_hi(ks1)};
    *reinterpret_cast<f32x2*>(lw + L_KP + 1536 + lane * 8) = f32x2{-bf_lo(kz1) * bf_lo(ks1), -bf_hi(kz1) * bf_hi(ks1)};
    *reinterpret_cast<f32x4*>(lw + L_VS + lane * 16) = f32x4{bf_lo(vsr[0]), bf_hi(vsr[0]), bf_lo(vsr[1]), bf_hi(vsr[1])};
    *reinterpret_cast<u32x2*>(lw + L_VZ + lane * 8) = u32x2{vzr[0] ^ 0x80008000u, vzr[1] ^ 0x80008000u};
  }
  SB();

  unsigned char* lw = lds + wave * L_WAVE;
  unsigned char* kt_w = lw + L_KT + g * (4 * KROW) + c * 8;
  const unsigned char* kt_r = lw + L_KT + c * KROW + g * 16;
  const u32 lomask = c >= 8 ? 0xffff0000u : 0u;
  f32x4 acc[2][4], accz = {0.f, 0.f, 0.f, 0.f};
#pragma unroll
  for (int h = 0; h < 2; ++h)
#pragma unroll
    for (int e = 0; e < 4; ++e) acc[h][e] = f32x4{0.f, 0.f, 0.f, 0.f};
  float m = -INFINITY, l = 0.f;

#pragma unroll 1
  for (int it = 0; it < NT; ++it) {
    const bool more = it + 1 < NT;
    const int cur = (it + rot) & (NT - 1);
    const int soff = ((cur + 1) & (NT - 1)) * 8192;
    const rsrc_t rk = __builtin_amdgcn_make_buffer_rsrc((void*)kb, 0, more ? TPW * DH * 4 : 0, 0x00020000);
    const rsrc_t rv = __builtin_amdgcn_make_buffer_rsrc((void*)vb, 0, more ? TPW * DH * 4 : 0, 0x00020000);
    const unsigned char* kp = lw + L_KP + (cur >> 3) * 1024 + c * 16;
    f32x4 sc[2], zc[2];
#pragma unroll
    for (int h = 0; h < 2; ++h) { sc[h] = *reinterpret_cast<const f32x4*>(kp + h * 256); zc[h] = *reinterpret_cast<const f32x4*>(kp + 512 + h * 256); }
    const f32x4 vsc = *reinterpret_cast<const f32x4*>(lw + L_VS + cur * 64 + g * 16);
    const u32x2 vzn = *reinterpret_cast<const u32x2*>(lw + L_VZ + cur * 32 + g * 8);
    SB();
#pragma unroll
    for (int n = 0; n < 8; ++n) {
      const int h = n & 1;
      const u32x2 w = {pk_bf16(fmaf(i2f(K[n][0]), sc[h][0], zc[h][0]), fmaf(i2f(K[n][1]), sc[h][1], zc[h][1])),
                       pk_bf16(fmaf(i2f(K[n][2]), sc[h][2], zc[h][2]), fmaf(i2f(K[n][3]), sc[h][3], zc[h][3]))};
      *reinterpret_cast<u32x2*>(kt_w + (n >> 1) * KROW + h * 128) = w;
      SB();
      K[n] = __builtin_amdgcn_raw_buffer_load_b128(rk, voff + (n >> 1) * 512 + h * 256, soff, AUXK);
      SB();
    }
    bf16x8 kf[4];
#pragma unroll
    for (int s = 0; s < 4; ++s) kf[s] = __builtin_bit_cast(bf16x8, *reinterpret_cast<const u32x4*>(kt_r + 64 * s));
    SB();
    u32x2 vf[2][4];
#pragma unroll
    for (int mp = 0; mp < 2; ++mp)
#pragma unroll
      for (int h = 0; h < 2; ++h) {
        const int n0 = 4 * mp + h, n1 = n0 + 2;
#pragma unroll
        for (int e = 0; e < 4; ++e) vf[h][e][mp] = pk_bf16(i2f(V[n0][e]), i2f(V[n1][e]));
        SB();
        V[n0] = __builtin_amdgcn_raw_buffer_load_b128(rv, voff + (n0 >> 1) * 512 + h * 256, soff, AUXV);
        V[n1] = __builtin_amdgcn_raw_buffer_load_b128(rv, voff + (n1 >> 1) * 512 + h * 256, soff, AUXV);
        SB();
      }
    f32x4 S = {0.f, 0.f, 0.f, 0.f};
#pragma unroll
    for (int s = 0; s < 4; ++s) S = __builtin_amdgcn_mfma_f32_16x16x32_bf16(kf[s], qf[s], S, 0, 0, 0);
    const float mn = fmaxf(m, xrow_max(fmaxf(fmaxf(S[0], S[1]), fmaxf(S[2], S[3]))));
    const float alpha = __builtin_amdgcn_exp2f((m - mn) * QSCALE);
    const float mc = mn * QSCALE;
    m = mn;
    float p[4];
#pragma unroll
    for (int r = 0; r < 4; ++r) p[r] = __builtin_amdgcn_exp2f(fmaf(S[r], QSCALE, -mc));
    l = fmaf(l, alpha, (p[0] + p[1]) + (p[2] + p[3]));
    u32 y[4];
#pragma unroll
    for (int r = 0; r < 4; ++r) {
      const float x = p[r] * vsc[r];
      y[r] = __float_as_uint(x - __uint_as_float(__float_as_uint(x) & lomask));
    }
    const u32x2 pfu = {__builtin_amdgcn_perm(y[1], y[0], 0x07060302u), __builtin_amdgcn_perm(y[3], y[2], 0x07060302u)};
    const s16x4 pf = __builtin_bit_cast(s16x4, pfu);
    accz = __builtin_amdgcn_mfma_f32_16x16x16bf16_1k(__builtin_bit_cast(s16x4, vzn), pf, accz * alpha, 0, 0, 0);
#pragma unroll
    for (int h = 0; h < 2; ++h)
#pragma unroll
      for (int e = 0; e < 4; ++e)
        acc[h][e] = __builtin_amdgcn_mfma_f32_16x16x16bf16_1k(__builtin_bit_cast(s16x4, vf[h][e]), pf, acc[h][e] * alpha, 0, 0, 0);
    SB();
  }
  STAMP(1);
  asm volatile("" :: "v"(otouch));
  const u32 tagA = epoch[0] + 1u, tagB = epoch[1] + 1u;
  const int ib = (split * NSPLIT + 8 * wave + g) * PKT;
  u32x4 x[2], y[2];
#pragma unroll
  for (int j = 0; j < 2; ++j) {
    x[j] = __builtin_amdgcn_raw_buffer_load_b128(rp, ib + j * 4 * PKT + c * 16, 0, AUX_SC1);
    y[j] = __builtin_amdgcn_raw_buffer_load_b128(rp, ib + j * 4 * PKT + 256, 0, AUX_SC1);
  }
  SB();

  l = xrow_sum(l);
  {
    const bool up = c >= 8;
    unsigned char* orow = lds + L_MRG + (wave * REP + (c & 7)) * OROW + (up ? 256 : 0) + g * 64;
#pragma unroll
    for (int r = 0; r < 4; ++r) {
      f32x4 o;
      const float z = accz[r] + dpp<0x128>(accz[r]);
#pragma unroll
      for (int e = 0; e < 4; ++e) {
        const float own = up ? acc[1][e][r] : acc[0][e][r], oth = up ? acc[0][e][r] : acc[1][e][r];
        o[e] = own + dpp<0x128>(oth) + z;
      }
      *reinterpret_cast<f32x4*>(orow + r * 16) = o;
    }
    if (lane < 8) *reinterpret_cast<f32x2*>(lds + L_ML + (wave * REP + lane) * 8) = f32x2{m * QSCALE, l};
  }
  __syncthreads();
  STAMP(2);
  {
    const int h = tid >> 5, d4 = tid & 31;
    f32x2 ml[NW];
    float M = -INFINITY;
#pragma unroll
    for (int w = 0; w < NW; ++w) { ml[w] = *reinterpret_cast<const f32x2*>(lds + L_ML + (w * REP + h) * 8); M = fmaxf(M, ml[w][0]); }
    float L = 0.f; f32x4 o = {0.f, 0.f, 0.f, 0.f};
#pragma unroll
    for (int w = 0; w < NW; ++w) {
      const float wt = __builtin_amdgcn_exp2f(ml[w][0] - M);
      const f32x4 a = *reinterpret_cast<const f32x4*>(lds + L_MRG + (w * REP + h) * OROW + d4 * 16);
      L = fmaf(wt, ml[w][1], L); o += a * wt;
    }
    const int po = ((tid >> 3) * NSPLIT + split) * PKT;
    __builtin_amdgcn_raw_buffer_store_b128(u32x4{__float_as_uint(o[0]), tagA, __float_as_uint(o[1]), tagB}, rp, po + (tid & 7) * 16, 0, AUX_SC1);
    __builtin_amdgcn_raw_buffer_store_b128(u32x4{__float_as_uint(o[2]), tagA, __float_as_uint(o[3]), tagB}, rp, po + (tid & 7) * 16 + 128, 0, AUX_SC1);
    __builtin_amdgcn_raw_buffer_store_b128(u32x4{__float_as_uint(M), tagA, __float_as_uint(L), tagB}, rp,
        (tid & 7) == 0 ? po + 256 : OOB,
        0, AUX_SC1);
    SB();
  }
  STAMP(3);
  STAMP(5);
  {
    float d0[2], d1[2], pm[2], pl[2];
    bool pend[2] = {true, true};
#pragma unroll
    for (int j = 0; j < 2; ++j) { d0[j] = 0.f; d1[j] = 0.f; pm[j] = 0.f; pl[j] = 0.f; }
    for (int spins = 0;; ++spins) {
#pragma unroll
      for (int j = 0; j < 2; ++j) {
        const bool ok = pend[j] & ((((x[j][1] ^ tagA) | (x[j][3] ^ tagB)) | ((y[j][1] ^ tagA) | (y[j][3] ^ tagB))) == 0u);
        d0[j] = ok ? __uint_as_float(x[j][0]) : d0[j]; d1[j] = ok ? __uint_as_float(x[j][2]) : d1[j];
        pm[j] = ok ? __uint_as_float(y[j][0]) : pm[j]; pl[j] = ok ? __uint_as_float(y[j][2]) : pl[j];
        pend[j] = pend[j] & !ok;
      }
      const int nmiss = __builtin_popcountll(__builtin_amdgcn_ballot_w64(pend[0])) + __builtin_popcountll(__builtin_amdgcn_ballot_w64(pend[1]));
      if (nmiss == 0 || spins > (1 << 20)) break;
#ifdef LONG_FIRST
      if (spins == 0) { for (int z = 0; z < LONG_FIRST; ++z) __builtin_amdgcn_s_sleep(127); }
#endif
      if (spins > 0) { if (nmiss > 32) __builtin_amdgcn_s_sleep(SLEEP_FAR); else __builtin_amdgcn_s_sleep(SLEEP_NEAR); }
      asm volatile("" ::: "memory");
#pragma unroll
      for (int j = 0; j < 2; ++j) {
        x[j] = __builtin_amdgcn_raw_buffer_load_b128(rp, pend[j] ? ib + j * 4 * PKT + c * 16 : OOB, 0, AUX_SC1);
        y[j] = __builtin_amdgcn_raw_buffer_load_b128(rp, pend[j] ? ib + j * 4 * PKT + 256 : OOB, 0, AUX_SC1);
      }
    }
    const float M = xrow_max(fmaxf(pm[0], pm[1]));
    const float w0 = __builtin_amdgcn_exp2f(pm[0] - M), w1 = __builtin_amdgcn_exp2f(pm[1] - M);
    const float L = xrow_sum(fmaf(w0, pl[0], w1 * pl[1])), o0 = xrow_sum(fmaf(w0, d0[0], w1 * d0[1])), o1 = xrow_sum(fmaf(w0, d1[0], w1 * d1[1]));
    if (lane < 16) *reinterpret_cast<f32x4*>(lds + wave * L_WAVE + lane * 16) = f32x4{M, L, o0, o1};
    __syncthreads();
    if (tid < 16) {
      f32x4 r[NW];
#pragma unroll
      for (int w = 0; w < NW; ++w) r[w] = *reinterpret_cast<const f32x4*>(lds + w * L_WAVE + lane * 16);
      const float MM = fmaxf(fmaxf(r[0][0], r[1][0]), fmaxf(r[2][0], r[3][0]));
      float LL = 0.f, a0 = 0.f, a1 = 0.f;
#pragma unroll
      for (int w = 0; w < NW; ++w) { const float wt = __builtin_amdgcn_exp2f(r[w][0] - MM); LL = fmaf(wt, r[w][1], LL); a0 = fmaf(wt, r[w][2], a0); a1 = fmaf(wt, r[w][3], a1); }
      const float inv = __builtin_amdgcn_rcpf(LL);
      outw[2 * (c & 7) + (c >> 3)] = pk_bf16(a0 * inv, a1 * inv);
      if (split == 0 && tid == 0) __builtin_amdgcn_raw_buffer_store_b64(u32x2{tagA, tagB}, rp, 0, WS_EPOCH, AUX_SC1);
    }
  }
}

extern "C" void kernel_launch(void* const* d_in, const int* in_sizes, int n_in, void* d_out, int out_size,
                              void* d_ws, size_t ws_size, hipStream_t stream) {
  (void)hipFuncSetAttribute((const void*)kivi_split, hipFuncAttributeMaxDynamicSharedMemorySize, LDS_BYTES);
  kivi_split<<<NBH * NSPLIT, NW * 64, LDS_BYTES, stream>>>((const u32*)d_in[0], (const int*)d_in[1], (const int*)d_in[2],
      (const u32*)d_in[3], (const u32*)d_in[4], (const u32*)d_in[5], (const u32*)d_in[6], (unsigned char*)d_ws, (u32*)d_out);
}
```

```cpp
#include <hip/hip_runtime.h>
#include <hip/hip_bf16.h>
#include <stdint.h>
#include <stdio.h>
#define PT_IN 1

typedef unsigned u32;
typedef u32 u32x4 __attribute__((ext_vector_type(4)));
typedef u32 u32x2 __attribute__((ext_vector_type(2)));
typedef float f32x4 __attribute__((ext_vector_type(4)));
typedef float f32x2 __attribute__((ext_vector_type(2)));
typedef __bf16 bf16x8 __attribute__((ext_vector_type(8)));
typedef __bf16 bf16x2 __attribute__((ext_vector_type(2)));
typedef short s16x4 __attribute__((ext_vector_type(4)));
typedef __amdgpu_buffer_rsrc_t rsrc_t;

#define AUXK 2
#define AUXV 2
constexpr int B_ = 2, HQ = 32, HKV = 4, SKV = 32768, DH = 128, GT = 128, NGK = SKV / GT;
constexpr int REP = HQ / HKV;
constexpr int NBH = B_ * HKV;
constexpr int NW = 4;
constexpr int NSPLIT = 32;
constexpr int TPW = SKV / NSPLIT / NW;
constexpr int NT = TPW / 16;
constexpr float QSCALE = 0.088388347648318440f * 1.4426950408889634f;

constexpr int KROW = 272, L_KT = 0, L_KP = 16 * KROW, L_VS = L_KP + 2048, L_VZ = L_VS + 1024, L_WAVE = L_VZ + 512;
constexpr int OROW = (DH + 4) * 4;
constexpr int L_MRG = NW * L_WAVE, L_ML = L_MRG + NW * REP * OROW, L_END = L_ML + NW * REP * 8;
constexpr int LDS_BYTES = 84 * 1024;
static_assert(L_END <= LDS_BYTES, "LDS map");

#define STAMP(i)

__device__ __forceinline__ float bf_lo(u32 x) { return __builtin_bit_cast(float, x << 16); }
__device__ __forceinline__ float bf_hi(u32 x) { return __builtin_bit_cast(float, x & 0xffff0000u); }
__device__ __forceinline__ u32 pk_bf16(float a, float b) {
  bf16x2 r; r[0] = (__bf16)a; r[1] = (__bf16)b; return __builtin_bit_cast(u32, r);
}
__device__ __forceinline__ float i2f(u32 x) { return (float)(int)x; }
template <int CTRL> __device__ __forceinline__ float dpp(float x) {
  return __builtin_bit_cast(float, __builtin_amdgcn_mov_dpp(__builtin_bit_cast(int, x), CTRL, 0xf, 0xf, true));
}
__device__ __forceinline__ float xrow_max(float x) {
  auto s = __builtin_amdgcn_permlane16_swap(__float_as_uint(x), __float_as_uint(x), false, false);
  x = fmaxf(__uint_as_float(s[0]), __uint_as_float(s[1]));
  auto t = __builtin_amdgcn_permlane32_swap(__float_as_uint(x), __float_as_uint(x), false, false);
  return fmaxf(__uint_as_float(t[0]), __uint_as_float(t[1]));
}
__device__ __forceinline__ float xrow_sum(float x) {
  auto s = __builtin_amdgcn_permlane16_swap(__float_as_uint(x), __float_as_uint(x), false, false);
  x = __uint_as_float(s[0]) + __uint_as_float(s[1]);
  auto t = __builtin_amdgcn_permlane32_swap(__float_as_uint(x), __float_as_uint(x), false, false);
  return __uint_as_float(t[0]) + __uint_as_float(t[1]);
}
#define SB() __builtin_amdgcn_sched_barrier(0)

constexpr int PKT = 384, PKT_ALL = NSPLIT * NSPLIT * PKT, WS_GRP = 512 * 1024, WS_EPOCH = PKT_ALL;
constexpr int OOB = 0x7fffffe0;
constexpr int AUX_SC1 = 16;
#define ROT 3
#define SMALL_FIRST 1
#define SLEEP_FAR 16
#define SLEEP_NEAR 1

__global__ void __launch_bounds__(NW * 64, 2)
kivi_split(const u32* __restrict__ q, const int* __restrict__ qk, const int* __restrict__ qv,
           const u32* __restrict__ ks, const u32* __restrict__ kz,
           const u32* __restrict__ vs, const u32* __restrict__ vz, unsigned char* __restrict__ ws, u32* __restrict__ out) {
  extern __shared__ __attribute__((aligned(16))) unsigned char lds[];
  STAMP(0);
  const int tid = threadIdx.x, lane = tid & 63;
  const int wave = __builtin_amdgcn_readfirstlane(tid >> 6);
  const int c = lane & 15, g = lane >> 4;
  const int bh = blockIdx.x & 7, split = blockIdx.x >> 3;
  const int tw = (split * NW + wave) * TPW;
  const size_t tok0 = (size_t)bh * SKV + tw;
  const char* kb = (const char*)qk + tok0 * (DH * 4);
  const char* vb = (const char*)qv + tok0 * (DH * 4);
  const int voff = g * 2048 + c * 16;
  const int rot = ((blockIdx.x * NW + wave) * ROT) & (NT - 1);
  u32x4 K[8], V[8];
  bf16x8 qf[4];
  u32x2 epoch;
  u32 otouch;
  const rsrc_t rp = __builtin_amdgcn_make_buffer_rsrc((void*)(ws + (size_t)bh * WS_GRP), 0, WS_EPOCH + 8, 0x00020000);
  u32* outw = out + ((bh * REP + (split >> 2)) * DH + (split & 3) * 32) / 2;
  {
    const rsrc_t rk = __builtin_amdgcn_make_buffer_rsrc((void*)kb, 0, TPW * DH * 4, 0x00020000);
    const rsrc_t rv = __builtin_amdgcn_make_buffer_rsrc((void*)vb, 0, TPW * DH * 4, 0x00020000);
    unsigned char* lw = lds + wave * L_WAVE;
    const size_t grow = ((size_t)bh * NGK + tw / GT) * (DH / 2);
    const u32 ks0 = ks[grow + lane], kz0 = kz[grow + lane], ks1 = ks[grow + 64 + lane], kz1 = kz[grow + 64 + lane];
    const u32x2 vsr = *reinterpret_cast<const u32x2*>(vs + tok0 / 2 + 2 * lane);
    const u32x2 vzr = *reinterpret_cast<const u32x2*>(vz + tok0 / 2 + 2 * lane);
    SB();
#pragma unroll
    for (int n = 0; n < 8; ++n) K[n] = __builtin_amdgcn_raw_buffer_load_b128(rk, voff + (n >> 1) * 512 + (n & 1) * 256, rot * 8192, AUXK);
    SB();
#pragma unroll
    for (int i = 0; i < 8; ++i) { const int n = (i & 4) + ((i & 1) << 1) + ((i >> 1) & 1);
      V[n] = __builtin_amdgcn_raw_buffer_load_b128(rv, voff + (n >> 1) * 512 + (n & 1) * 256, rot * 8192, AUXV); }
    SB();
    const u32* qp = q + ((size_t)bh * REP + (c & 7)) * (DH / 2) + 4 * g;
#pragma unroll
    for (int s = 0; s < 4; ++s) qf[s] = __builtin_bit_cast(bf16x8, *reinterpret_cast<const u32x4*>(qp + 16 * s));
    epoch = __builtin_amdgcn_raw_buffer_load_b64(rp, 0, WS_EPOCH, AUX_SC1);
    otouch = __builtin_amdgcn_raw_buffer_load_b32(rp, (split * NSPLIT + 8 * wave + (lane >> 3)) * PKT + (lane & 3) * 128, 0, 16);
    otouch += outw[c];
    SB();
    *reinterpret_cast<f32x2*>(lw + L_KP + lane * 8) = f32x2{bf_lo(ks0), bf_hi(ks0)};
    *reinterpret_cast<f32x2*>(lw + L_KP + 512 + lane * 8) = f32x2{-bf_lo(kz0) * bf_lo(ks0), -bf_hi(kz0) * bf_hi(ks0)};
    *reinterpret_cast<f32x2*>(lw + L_KP + 1024 + lane * 8) = f32x2{bf_lo(ks1), bf_hi(ks1)};
    *reinterpret_cast<f32x2*>(lw + L_KP + 1536 + lane * 8) = f32x2{-bf_lo(kz1) * bf_lo(ks1), -bf_hi(kz1) * bf_hi(ks1)};
    *reinterpret_cast<f32x4*>(lw + L_VS + lane * 16) = f32x4{bf_lo(vsr[0]), bf_hi(vsr[0]), bf_lo(vsr[1]), bf_hi(vsr[1])};
    *reinterpret_cast<u32x2*>(lw + L_VZ + lane * 8) = u32x2{vzr[0] ^ 0x80008000u, vzr[1] ^ 0x80008000u};
  }
  SB();

  unsigned char* lw = lds + wave * L_WAVE;
  unsigned char* kt_w = lw + L_KT + g * (4 * KROW) + c * 8;
  const unsigned char* kt_r = lw + L_KT + c * KROW + g * 16;
  const u32 lomask = c >= 8 ? 0xffff0000u : 0u;
  f32x4 acc[2][4], accz = {0.f, 0.f, 0.f, 0.f};
#pragma unroll
  for (int h = 0; h < 2; ++h)
#pragma unroll
    for (int e = 0; e < 4; ++e) acc[h][e] = f32x4{0.f, 0.f, 0.f, 0.f};
  float m = -INFINITY, l = 0.f;

#pragma unroll 1
  for (int it = 0; it < NT; ++it) {
    const bool more = it + 1 < NT;
    const int cur = (it + rot) & (NT - 1);
    const int soff = ((cur + 1) & (NT - 1)) * 8192;
    const rsrc_t rk = __builtin_amdgcn_make_buffer_rsrc((void*)kb, 0, more ? TPW * DH * 4 : 0, 0x00020000);
    const rsrc_t rv = __builtin_amdgcn_make_buffer_rsrc((void*)vb, 0, more ? TPW * DH * 4 : 0, 0x00020000);
    const unsigned char* kp = lw + L_KP + (cur >> 3) * 1024 + c * 16;
    f32x4 sc[2], zc[2];
#pragma unroll
    for (int h = 0; h < 2; ++h) { sc[h] = *reinterpret_cast<const f32x4*>(kp + h * 256); zc[h] = *reinterpret_cast<const f32x4*>(kp + 512 + h * 256); }
    const f32x4 vsc = *reinterpret_cast<const f32x4*>(lw + L_VS + cur * 64 + g * 16);
    const u32x2 vzn = *reinterpret_cast<const u32x2*>(lw + L_VZ + cur * 32 + g * 8);
    SB();
#pragma unroll
    for (int n = 0; n < 8; ++n) {
      const int h = n & 1;
      const u32x2 w = {pk_bf16(fmaf(i2f(K[n][0]), sc[h][0], zc[h][0]), fmaf(i2f(K[n][1]), sc[h][1], zc[h][1])),
                       pk_bf16(fmaf(i2f(K[n][2]), sc[h][2], zc[h][2]), fmaf(i2f(K[n][3]), sc[h][3], zc[h][3]))};
      *reinterpret_cast<u32x2*>(kt_w + (n >> 1) * KROW + h * 128) = w;
      SB();
      K[n] = __builtin_amdgcn_raw_buffer_load_b128(rk, voff + (n >> 1) * 512 + h * 256, soff, AUXK);
      SB();
    }
    bf16x8 kf[4];
#pragma unroll
    for (int s = 0; s < 4; ++s) kf[s] = __builtin_bit_cast(bf16x8, *reinterpret_cast<const u32x4*>(kt_r + 64 * s));
    SB();
    u32x2 vf[2][4];
#pragma unroll
    for (int mp = 0; mp < 2; ++mp)
#pragma unroll
      for (int h = 0; h < 2; ++h) {
        const int n0 = 4 * mp + h, n1 = n0 + 2;
#pragma unroll
        for (int e = 0; e < 4; ++e) vf[h][e][mp] = pk_bf16(i2f(V[n0][e]), i2f(V[n1][e]));
        SB();
        V[n0] = __builtin_amdgcn_raw_buffer_load_b128(rv, voff + (n0 >> 1) * 512 + h * 256, soff, AUXV);
        V[n1] = __builtin_amdgcn_raw_buffer_load_b128(rv, voff + (n1 >> 1) * 512 + h * 256, soff, AUXV);
        SB();
      }
    f32x4 S = {0.f, 0.f, 0.f, 0.f};
#pragma unroll
    for (int s = 0; s < 4; ++s) S = __builtin_amdgcn_mfma_f32_16x16x32_bf16(kf[s], qf[s], S, 0, 0, 0);
    const float mn = fmaxf(m, xrow_max(fmaxf(fmaxf(S[0], S[1]), fmaxf(S[2], S[3]))));
    const float alpha = __builtin_amdgcn_exp2f((m - mn) * QSCALE);
    const float mc = mn * QSCALE;
    m = mn;
    float p[4];
#pragma unroll
    for (int r = 0; r < 4; ++r) p[r] = __builtin_amdgcn_exp2f(fmaf(S[r], QSCALE, -mc));
    l = fmaf(l, alpha, (p[0] + p[1]) + (p[2] + p[3]));
    u32 y[4];
#pragma unroll
    for (int r = 0; r < 4; ++r) {
      const float x = p[r] * vsc[r];
      y[r] = __float_as_uint(x - __uint_as_float(__float_as_uint(x) & lomask));
    }
    const u32x2 pfu = {__builtin_amdgcn_perm(y[1], y[0], 0x07060302u), __builtin_amdgcn_perm(y[3], y[2], 0x07060302u)};
    const s16x4 pf = __builtin_bit_cast(s16x4, pfu);
    accz = __builtin_amdgcn_mfma_f32_16x16x16bf16_1k(__builtin_bit_cast(s16x4, vzn), pf, accz * alpha, 0, 0, 0);
#pragma unroll
    for (int h = 0; h < 2; ++h)
#pragma unroll
      for (int e = 0; e < 4; ++e)
        acc[h][e] = __builtin_amdgcn_mfma_f32_16x16x16bf16_1k(__builtin_bit_cast(s16x4, vf[h][e]), pf, acc[h][e] * alpha, 0, 0, 0);
    SB();
  }
  STAMP(1);
  asm volatile("" :: "v"(otouch));
  const u32 tagA = epoch[0] + 1u, tagB = epoch[1] + 1u;
  const int ib = (split * NSPLIT + 8 * wave + g) * PKT;
  u32x4 x[2], y[2];
#pragma unroll
  for (int j = 0; j < 2; ++j) {
    x[j] = u32x4{0u, tagA + 1u, 0u, 0u}; y[j] = x[j];
  }
  SB();

  l = xrow_sum(l);
  {
    const bool up = c >= 8;
    unsigned char* orow = lds + L_MRG + (wave * REP + (c & 7)) * OROW + (up ? 256 : 0) + g * 64;
#pragma unroll
    for (int r = 0; r < 4; ++r) {
      f32x4 o;
      const float z = accz[r] + dpp<0x128>(accz[r]);
#pragma unroll
      for (int e = 0; e < 4; ++e) {
        const float own = up ? acc[1][e][r] : acc[0][e][r], oth = up ? acc[0][e][r] : acc[1][e][r];
        o[e] = own + dpp<0x128>(oth) + z;
      }
      *reinterpret_cast<f32x4*>(orow + r * 16) = o;
    }
    if (lane < 8) *reinterpret_cast<f32x2*>(lds + L_ML + (wave * REP + lane) * 8) = f32x2{m * QSCALE, l};
  }
  __syncthreads();
  STAMP(2);
  {
    const int h = tid >> 5, d4 = tid & 31;
    f32x2 ml[NW];
    float M = -INFINITY;
#pragma unroll
    for (int w = 0; w < NW; ++w) { ml[w] = *reinterpret_cast<const f32x2*>(lds + L_ML + (w * REP + h) * 8); M = fmaxf(M, ml[w][0]); }
    float L = 0.f; f32x4 o = {0.f, 0.f, 0.f, 0.f};
#pragma unroll
    for (int w = 0; w < NW; ++w) {
      const float wt = __builtin_amdgcn_exp2f(ml[w][0] - M);
      const f32x4 a = *reinterpret_cast<const f32x4*>(lds + L_MRG + (w * REP + h) * OROW + d4 * 16);
      L = fmaf(wt, ml[w][1], L); o += a * wt;
    }
    const int po = ((tid >> 3) * NSPLIT + split) * PKT;
    __builtin_amdgcn_raw_buffer_store_b128(u32x4{__float_as_uint(o[0]), tagA, __float_as_uint(o[1]), tagB}, rp, po + (tid & 7) * 16, 0, AUX_SC1);
    __builtin_amdgcn_raw_buffer_store_b128(u32x4{__float_as_uint(o[2]), tagA, __float_as_uint(o[3]), tagB}, rp, po + (tid & 7) * 16 + 128, 0, AUX_SC1);
    __builtin_amdgcn_raw_buffer_store_b128(u32x4{__float_as_uint(M), tagA, __float_as_uint(L), tagB}, rp,
        (tid & 7) == 0 ? po + 256 : OOB,
        0, AUX_SC1);
    SB();
  }
  STAMP(3);
  STAMP(5);
  {
    float d0[2], d1[2], pm[2], pl[2];
    bool pend[2] = {true, true};
#pragma unroll
    for (int j = 0; j < 2; ++j) { d0[j] = 0.f; d1[j] = 0.f; pm[j] = 0.f; pl[j] = 0.f; }
    for (int spins = 0;; ++spins) {
#pragma unroll
      for (int j = 0; j < 2; ++j) {
        const bool ok = pend[j] & ((((x[j][1] ^ tagA) | (x[j][3] ^ tagB)) | ((y[j][1] ^ tagA) | (y[j][3] ^ tagB))) == 0u);
        d0[j] = ok ? __uint_as_float(x[j][0]) : d0[j]; d1[j] = ok ? __uint_as_float(x[j][2]) : d1[j];
        pm[j] = ok ? __uint_as_float(y[j][0]) : pm[j]; pl[j] = ok ? __uint_as_float(y[j][2]) : pl[j];
        pend[j] = pend[j] & !ok;
      }
      const int nmiss = __builtin_popcountll(__builtin_amdgcn_ballot_w64(pend[0])) + __builtin_popcountll(__builtin_amdgcn_ballot_w64(pend[1]));
      if (nmiss == 0 || spins > (1 << 20)) break;
#ifdef LONG_FIRST
      if (spins == 0) { for (int z = 0; z < LONG_FIRST; ++z) __builtin_amdgcn_s_sleep(127); }
#endif
      if (spins > 0) { if (nmiss > 32) __builtin_amdgcn_s_sleep(SLEEP_FAR); else __builtin_amdgcn_s_sleep(SLEEP_NEAR); }
      asm volatile("" ::: "memory");
#pragma unroll
      for (int j = 0; j < 2; ++j) {
        x[j] = __builtin_amdgcn_raw_buffer_load_b128(rp, pend[j] ? ib + j * 4 * PKT + c * 16 : OOB, 0, AUX_SC1);
        y[j] = __builtin_amdgcn_raw_buffer_load_b128(rp, pend[j] ? ib + j * 4 * PKT + 256 : OOB, 0, AUX_SC1);
      }
    }
    const float M = xrow_max(fmaxf(pm[0], pm[1]));
    const float w0 = __builtin_amdgcn_exp2f(pm[0] - M), w1 = __builtin_amdgcn_exp2f(pm[1] - M);
    const float L = xrow_sum(fmaf(w0, pl[0], w1 * pl[1])), o0 = xrow_sum(fmaf(w0, d0[0], w1 * d0[1])), o1 = xrow_sum(fmaf(w0, d1[0], w1 * d1[1]));
    if (lane < 16) *reinterpret_cast<f32x4*>(lds + wave * L_WAVE + lane * 16) = f32x4{M, L, o0, o1};
    __syncthreads();
    if (tid < 16) {
      f32x4 r[NW];
#pragma unroll
      for (int w = 0; w < NW; ++w) r[w] = *reinterpret_cast<const f32x4*>(lds + w * L_WAVE + lane * 16);
      const float MM = fmaxf(fmaxf(r[0][0], r[1][0]), fmaxf(r[2][0], r[3][0]));
      float LL = 0.f, a0 = 0.f, a1 = 0.f;
#pragma unroll
      for (int w = 0; w < NW; ++w) { const float wt = __builtin_amdgcn_exp2f(r[w][0] - MM); LL = fmaf(wt, r[w][1], LL); a0 = fmaf(wt, r[w][2], a0); a1 = fmaf(wt, r[w][3], a1); }
      const float inv = __builtin_amdgcn_rcpf(LL);
      outw[2 * (c & 7) + (c >> 3)] = pk_bf16(a0 * inv, a1 * inv);
      if (split == 0 && tid == 0) __builtin_amdgcn_raw_buffer_store_b64(u32x2{tagA, tagB}, rp, 0, WS_EPOCH, AUX_SC1);
    }
  }
}

extern "C" void kernel_launch(void* const* d_in, const int* in_sizes, int n_in, void* d_out, int out_size,
                              void* d_ws, size_t ws_size, hipStream_t stream) {
  fprintf(stderr, "PTRS q=%p k=%p v=%p ks=%p kz=%p vs=%p vz=%p out=%p ws=%p\n", d_in[0], d_in[1], d_in[2], d_in[3], d_in[4], d_in[5], d_in[6], d_out, d_ws);
  { hipPointerAttribute_t at; if (hipPointerGetAttributes(&at, d_in[1]) == hipSuccess) fprintf(stderr, "ATTR type=%d flags=%u managed=%d\n", (int)at.type, at.allocationFlags, at.isManaged); }
  (void)hipFuncSetAttribute((const void*)kivi_split, hipFuncAttributeMaxDynamicSharedMemorySize, LDS_BYTES);
  kivi_split<<<NBH * NSPLIT, NW * 64, LDS_BYTES, stream>>>((const u32*)d_in[0], (const int*)d_in[1], (const int*)d_in[2],
      (const u32*)d_in[3], (const u32*)d_in[4], (const u32*)d_in[5], (const u32*)d_in[6], (unsigned char*)d_ws, (u32*)d_out);
}
```

```cpp
#include <hip/hip_runtime.h>
#include <hip/hip_bf16.h>
#include <stdint.h>
#define PAD_END 100
#define PT_IN 1

typedef unsigned u32;
typedef u32 u32x4 __attribute__((ext_vector_type(4)));
typedef u32 u32x2 __attribute__((ext_vector_type(2)));
typedef float f32x4 __attribute__((ext_vector_type(4)));
typedef float f32x2 __attribute__((ext_vector_type(2)));
typedef __bf16 bf16x8 __attribute__((ext_vector_type(8)));
typedef __bf16 bf16x2 __attribute__((ext_vector_type(2)));
typedef short s16x4 __attribute__((ext_vector_type(4)));
typedef __amdgpu_buffer_rsrc_t rsrc_t;

#define AUXK 2
#define AUXV 2
constexpr int B_ = 2, HQ = 32, HKV = 4, SKV = 32768, DH = 128, GT = 128, NGK = SKV / GT;
constexpr int REP = HQ / HKV;
constexpr int NBH = B_ * HKV;
constexpr int NW = 4;
constexpr int NSPLIT = 32;
constexpr int TPW = SKV / NSPLIT / NW;
constexpr int NT = TPW / 16;
constexpr float QSCALE = 0.088388347648318440f * 1.4426950408889634f;

constexpr int KROW = 272, L_KT = 0, L_KP = 16 * KROW, L_VS = L_KP + 2048, L_VZ = L_VS + 1024, L_WAVE = L_VZ + 512;
constexpr int OROW = (DH + 4) * 4;
constexpr int L_MRG = NW * L_WAVE, L_ML = L_MRG + NW * REP * OROW, L_END = L_ML + NW * REP * 8;
constexpr int LDS_BYTES = 84 * 1024;
static_assert(L_END <= LDS_BYTES, "LDS map");

#define STAMP(i)

__device__ __forceinline__ float bf_lo(u32 x) { return __builtin_bit_cast(float, x << 16); }
__device__ __forceinline__ float bf_hi(u32 x) { return __builtin_bit_cast(float, x & 0xffff0000u); }
__device__ __forceinline__ u32 pk_bf16(float a, float b) {
  bf16x2 r; r[0] = (__bf16)a; r[1] = (__bf16)b; return __builtin_bit_cast(u32, r);
}
__device__ __forceinline__ float i2f(u32 x) { return (float)(int)x; }
template <int CTRL> __device__ __forceinline__ float dpp(float x) {
  return __builtin_bit_cast(float, __builtin_amdgcn_mov_dpp(__builtin_bit_cast(int, x), CTRL, 0xf, 0xf, true));
}
__device__ __forceinline__ float xrow_max(float x) {
  auto s = __builtin_amdgcn_permlane16_swap(__float_as_uint(x), __float_as_uint(x), false, false);
  x = fmaxf(__uint_as_float(s[0]), __uint_as_float(s[1]));
  auto t = __builtin_amdgcn_permlane32_swap(__float_as_uint(x), __float_as_uint(x), false, false);
  return fmaxf(__uint_as_float(t[0]), __uint_as_float(t[1]));
}
__device__ __forceinline__ float xrow_sum(float x) {
  auto s = __builtin_amdgcn_permlane16_swap(__float_as_uint(x), __float_as_uint(x), false, false);
  x = __uint_as_float(s[0]) + __uint_as_float(s[1]);
  auto t = __builtin_amdgcn_permlane32_swap(__float_as_uint(x), __float_as_uint(x), false, false);
  return __uint_as_float(t[0]) + __uint_as_float(t[1]);
}
#define SB() __builtin_amdgcn_sched_barrier(0)

constexpr int PKT = 384, PKT_ALL = NSPLIT * NSPLIT * PKT, WS_GRP = 512 * 1024, WS_EPOCH = PKT_ALL;
constexpr int OOB = 0x7fffffe0;
constexpr int AUX_SC1 = 16;
#define ROT 3
#define SMALL_FIRST 1
#define SLEEP_FAR 16
#define SLEEP_NEAR 1

__global__ void __launch_bounds__(NW * 64, 2)
kivi_split(const u32* __restrict__ q, const int* __restrict__ qk, const int* __restrict__ qv,
           const u32* __restrict__ ks, const u32* __restrict__ kz,
           const u32* __restrict__ vs, const u32* __restrict__ vz, unsigned char* __restrict__ ws, u32* __restrict__ out) {
  extern __shared__ __attribute__((aligned(16))) unsigned char lds[];
  STAMP(0);
  const unsigned long long tstart = __builtin_amdgcn_s_memrealtime();
  const int tid = threadIdx.x, lane = tid & 63;
  const int wave = __builtin_amdgcn_readfirstlane(tid >> 6);
  const int c = lane & 15, g = lane >> 4;
  const int bh = blockIdx.x & 7, split = blockIdx.x >> 3;
  const int tw = (split * NW + wave) * TPW;
  const size_t tok0 = (size_t)bh * SKV + tw;
  const char* kb = (const char*)qk + tok0 * (DH * 4);
  const char* vb = (const char*)qv + tok0 * (DH * 4);
  const int voff = g * 2048 + c * 16;
  const int rot = ((blockIdx.x * NW + wave) * ROT) & (NT - 1);
  u32x4 K[8], V[8];
  bf16x8 qf[4];
  u32x2 epoch;
  u32 otouch;
  const rsrc_t rp = __builtin_amdgcn_make_buffer_rsrc((void*)(ws + (size_t)bh * WS_GRP), 0, WS_EPOCH + 8, 0x00020000);
  u32* outw = out + ((bh * REP + (split >> 2)) * DH + (split & 3) * 32) / 2;
  {
    const rsrc_t rk = __builtin_amdgcn_make_buffer_rsrc((void*)kb, 0, TPW * DH * 4, 0x00020000);
    const rsrc_t rv = __builtin_amdgcn_make_buffer_rsrc((void*)vb, 0, TPW * DH * 4, 0x00020000);
    unsigned char* lw = lds + wave * L_WAVE;
    const size_t grow = ((size_t)bh * NGK + tw / GT) * (DH / 2);
    const u32 ks0 = ks[grow + lane], kz0 = kz[grow + lane], ks1 = ks[grow + 64 + lane], kz1 = kz[grow + 64 + lane];
    const u32x2 vsr = *reinterpret_cast<const u32x2*>(vs + tok0 / 2 + 2 * lane);
    const u32x2 vzr = *reinterpret_cast<const u32x2*>(vz + tok0 / 2 + 2 * lane);
    SB();
#pragma unroll
    for (int n = 0; n < 8; ++n) K[n] = __builtin_amdgcn_raw_buffer_load_b128(rk, voff + (n >> 1) * 512 + (n & 1) * 256, rot * 8192, AUXK);
    SB();
#pragma unroll
    for (int i = 0; i < 8; ++i) { const int n = (i & 4) + ((i & 1) << 1) + ((i >> 1) & 1);
      V[n] = __builtin_amdgcn_raw_buffer_load_b128(rv, voff + (n >> 1) * 512 + (n & 1) * 256, rot * 8192, AUXV); }
    SB();
    const u32* qp = q + ((size_t)bh * REP + (c & 7)) * (DH / 2) + 4 * g;
#pragma unroll
    for (int s = 0; s < 4; ++s) qf[s] = __builtin_bit_cast(bf16x8, *reinterpret_cast<const u32x4*>(qp + 16 * s));
    epoch = __builtin_amdgcn_raw_buffer_load_b64(rp, 0, WS_EPOCH, AUX_SC1);
    otouch = __builtin_amdgcn_raw_buffer_load_b32(rp, (split * NSPLIT + 8 * wave + (lane >> 3)) * PKT + (lane & 3) * 128, 0, 16);
    otouch += outw[c];
    SB();
    *reinterpret_cast<f32x2*>(lw + L_KP + lane * 8) = f32x2{bf_lo(ks0), bf_hi(ks0)};
    *reinterpret_cast<f32x2*>(lw + L_KP + 512 + lane * 8) = f32x2{-bf_lo(kz0) * bf_lo(ks0), -bf_hi(kz0) * bf_hi(ks0)};
    *reinterpret_cast<f32x2*>(lw + L_KP + 1024 + lane * 8) = f32x2{bf_lo(ks1), bf_hi(ks1)};
    *reinterpret_cast<f32x2*>(lw + L_KP + 1536 + lane * 8) = f32x2{-bf_lo(kz1) * bf_lo(ks1), -bf_hi(kz1) * bf_hi(ks1)};
    *reinterpret_cast<f32x4*>(lw + L_VS + lane * 16) = f32x4{bf_lo(vsr[0]), bf_hi(vsr[0]), bf_lo(vsr[1]), bf_hi(vsr[1])};
    *reinterpret_cast<u32x2*>(lw + L_VZ + lane * 8) = u32x2{vzr[0] ^ 0x80008000u, vzr[1] ^ 0x80008000u};
  }
  SB();

  unsigned char* lw = lds + wave * L_WAVE;
  unsigned char* kt_w = lw + L_KT + g * (4 * KROW) + c * 8;
  const unsigned char* kt_r = lw + L_KT + c * KROW + g * 16;
  const u32 lomask = c >= 8 ? 0xffff0000u : 0u;
  f32x4 acc[2][4], accz = {0.f, 0.f, 0.f, 0.f};
#pragma unroll
  for (int h = 0; h < 2; ++h)
#pragma unroll
    for (int e = 0; e < 4; ++e) acc[h][e] = f32x4{0.f, 0.f, 0.f, 0.f};
  float m = -INFINITY, l = 0.f;

#pragma unroll 1
  for (int it = 0; it < NT; ++it) {
    const bool more = it + 1 < NT;
    const int cur = (it + rot) & (NT - 1);
    const int soff = ((cur + 1) & (NT - 1)) * 8192;
    const rsrc_t rk = __builtin_amdgcn_make_buffer_rsrc((void*)kb, 0, more ? TPW * DH * 4 : 0, 0x00020000);
    const rsrc_t rv = __builtin_amdgcn_make_buffer_rsrc((void*)vb, 0, more ? TPW * DH * 4 : 0, 0x00020000);
    const unsigned char* kp = lw + L_KP + (cur >> 3) * 1024 + c * 16;
    f32x4 sc[2], zc[2];
#pragma unroll
    for (int h = 0; h < 2; ++h) { sc[h] = *reinterpret_cast<const f32x4*>(kp + h * 256); zc[h] = *reinterpret_cast<const f32x4*>(kp + 512 + h * 256); }
    const f32x4 vsc = *reinterpret_cast<const f32x4*>(lw + L_VS + cur * 64 + g * 16);
    const u32x2 vzn = *reinterpret_cast<const u32x2*>(lw + L_VZ + cur * 32 + g * 8);
    SB();
#pragma unroll
    for (int n = 0; n < 8; ++n) {
      const int h = n & 1;
      const u32x2 w = {pk_bf16(fmaf(i2f(K[n][0]), sc[h][0], zc[h][0]), fmaf(i2f(K[n][1]), sc[h][1], zc[h][1])),
                       pk_bf16(fmaf(i2f(K[n][2]), sc[h][2], zc[h][2]), fmaf(i2f(K[n][3]), sc[h][3], zc[h][3]))};
      *reinterpret_cast<u32x2*>(kt_w + (n >> 1) * KROW + h * 128) = w;
      SB();
      K[n] = __builtin_amdgcn_raw_buffer_load_b128(rk, voff + (n >> 1) * 512 + h * 256, soff, AUXK);
      SB();
    }
    bf16x8 kf[4];
#pragma unroll
    for (int s = 0; s < 4; ++s) kf[s] = __builtin_bit_cast(bf16x8, *reinterpret_cast<const u32x4*>(kt_r + 64 * s));
    SB();
    u32x2 vf[2][4];
#pragma unroll
    for (int mp = 0; mp < 2; ++mp)
#pragma unroll
      for (int h = 0; h < 2; ++h) {
        const int n0 = 4 * mp + h, n1 = n0 + 2;
#pragma unroll
        for (int e = 0; e < 4; ++e) vf[h][e][mp] = pk_bf16(i2f(V[n0][e]), i2f(V[n1][e]));
        SB();
        V[n0] = __builtin_amdgcn_raw_buffer_load_b128(rv, voff + (n0 >> 1) * 512 + h * 256, soff, AUXV);
        V[n1] = __builtin_amdgcn_raw_buffer_load_b128(rv, voff + (n1 >> 1) * 512 + h * 256, soff, AUXV);
        SB();
      }
    f32x4 S = {0.f, 0.f, 0.f, 0.f};
#pragma unroll
    for (int s = 0; s < 4; ++s) S = __builtin_amdgcn_mfma_f32_16x16x32_bf16(kf[s], qf[s], S, 0, 0, 0);
    const float mn = fmaxf(m, xrow_max(fmaxf(fmaxf(S[0], S[1]), fmaxf(S[2], S[3]))));
    const float alpha = __builtin_amdgcn_exp2f((m - mn) * QSCALE);
    const float mc = mn * QSCALE;
    m = mn;
    float p[4];
#pragma unroll
    for (int r = 0; r < 4; ++r) p[r] = __builtin_amdgcn_exp2f(fmaf(S[r], QSCALE, -mc));
    l = fmaf(l, alpha, (p[0] + p[1]) + (p[2] + p[3]));
    u32 y[4];
#pragma unroll
    for (int r = 0; r < 4; ++r) {
      const float x = p[r] * vsc[r];
      y[r] = __float_as_uint(x - __uint_as_float(__float_as_uint(x) & lomask));
    }
    const u32x2 pfu = {__builtin_amdgcn_perm(y[1], y[0], 0x07060302u), __builtin_amdgcn_perm(y[3], y[2], 0x07060302u)};
    const s16x4 pf = __builtin_bit_cast(s16x4, pfu);
    accz = __builtin_amdgcn_mfma_f32_16x16x16bf16_1k(__builtin_bit_cast(s16x4, vzn), pf, accz * alpha, 0, 0, 0);
#pragma unroll
    for (int h = 0; h < 2; ++h)
#pragma unroll
      for (int e = 0; e < 4; ++e)
        acc[h][e] = __builtin_amdgcn_mfma_f32_16x16x16bf16_1k(__builtin_bit_cast(s16x4, vf[h][e]), pf, acc[h][e] * alpha, 0, 0, 0);
    SB();
  }
  STAMP(1);
  asm volatile("" :: "v"(otouch));
  const u32 tagA = epoch[0] + 1u, tagB = epoch[1] + 1u;
  const int ib = (split * NSPLIT + 8 * wave + g) * PKT;
  u32x4 x[2], y[2];
#pragma unroll
  for (int j = 0; j < 2; ++j) {
    x[j] = u32x4{0u, tagA + 1u, 0u, 0u}; y[j] = x[j];
  }
  SB();

  l = xrow_sum(l);
  {
    const bool up = c >= 8;
    unsigned char* orow = lds + L_MRG + (wave * REP + (c & 7)) * OROW + (up ? 256 : 0) + g * 64;
#pragma unroll
    for (int r = 0; r < 4; ++r) {
      f32x4 o;
      const float z = accz[r] + dpp<0x128>(accz[r]);
#pragma unroll
      for (int e = 0; e < 4; ++e) {
        const float own = up ? acc[1][e][r] : acc[0][e][r], oth = up ? acc[0][e][r] : acc[1][e][r];
        o[e] = own + dpp<0x128>(oth) + z;
      }
      *reinterpret_cast<f32x4*>(orow + r * 16) = o;
    }
    if (lane < 8) *reinterpret_cast<f32x2*>(lds + L_ML + (wave * REP + lane) * 8) = f32x2{m * QSCALE, l};
  }
  __syncthreads();
  STAMP(2);
  {
    const int h = tid >> 5, d4 = tid & 31;
    f32x2 ml[NW];
    float M = -INFINITY;
#pragma unroll
    for (int w = 0; w < NW; ++w) { ml[w] = *reinterpret_cast<const f32x2*>(lds + L_ML + (w * REP + h) * 8); M = fmaxf(M, ml[w][0]); }
    float L = 0.f; f32x4 o = {0.f, 0.f, 0.f, 0.f};
#pragma unroll
    for (int w = 0; w < NW; ++w) {
      const float wt = __builtin_amdgcn_exp2f(ml[w][0] - M);
      const f32x4 a = *reinterpret_cast<const f32x4*>(lds + L_MRG + (w * REP + h) * OROW + d4 * 16);
      L = fmaf(wt, ml[w][1], L); o += a * wt;
    }
    const int po = ((tid >> 3) * NSPLIT + split) * PKT;
    __builtin_amdgcn_raw_buffer_store_b128(u32x4{__float_as_uint(o[0]), tagA, __float_as_uint(o[1]), tagB}, rp, po + (tid & 7) * 16, 0, AUX_SC1);
    __builtin_amdgcn_raw_buffer_store_b128(u32x4{__float_as_uint(o[2]), tagA, __float_as_uint(o[3]), tagB}, rp, po + (tid & 7) * 16 + 128, 0, AUX_SC1);
    __builtin_amdgcn_raw_buffer_store_b128(u32x4{__float_as_uint(M), tagA, __float_as_uint(L), tagB}, rp,
        (tid & 7) == 0 ? po + 256 : OOB,
        0, AUX_SC1);
    SB();
  }
  STAMP(3);
  STAMP(5);
  {
    float d0[2], d1[2], pm[2], pl[2];
    bool pend[2] = {true, true};
#pragma unroll
    for (int j = 0; j < 2; ++j) { d0[j] = 0.f; d1[j] = 0.f; pm[j] = 0.f; pl[j] = 0.f; }
    for (int spins = 0;; ++spins) {
#pragma unroll
      for (int j = 0; j < 2; ++j) {
        const bool ok = pend[j] & ((((x[j][1] ^ tagA) | (x[j][3] ^ tagB)) | ((y[j][1] ^ tagA) | (y[j][3] ^ tagB))) == 0u);
        d0[j] = ok ? __uint_as_float(x[j][0]) : d0[j]; d1[j] = ok ? __uint_as_float(x[j][2]) : d1[j];
        pm[j] = ok ? __uint_as_float(y[j][0]) : pm[j]; pl[j] = ok ? __uint_as_float(y[j][2]) : pl[j];
        pend[j] = pend[j] & !ok;
      }
      const int nmiss = __builtin_popcountll(__builtin_amdgcn_ballot_w64(pend[0])) + __builtin_popcountll(__builtin_amdgcn_ballot_w64(pend[1]));
      if (nmiss == 0 || spins > (1 << 20)) break;
#ifdef LONG_FIRST
      if (spins == 0) { for (int z = 0; z < LONG_FIRST; ++z) __builtin_amdgcn_s_sleep(127); }
#endif
      if (spins > 0) { if (nmiss > 32) __builtin_amdgcn_s_sleep(SLEEP_FAR); else __builtin_amdgcn_s_sleep(SLEEP_NEAR); }
      asm volatile("" ::: "memory");
#pragma unroll
      for (int j = 0; j < 2; ++j) {
        x[j] = __builtin_amdgcn_raw_buffer_load_b128(rp, pend[j] ? ib + j * 4 * PKT + c * 16 : OOB, 0, AUX_SC1);
        y[j] = __builtin_amdgcn_raw_buffer_load_b128(rp, pend[j] ? ib + j * 4 * PKT + 256 : OOB, 0, AUX_SC1);
      }
    }
    const float M = xrow_max(fmaxf(pm[0], pm[1]));
    const float w0 = __builtin_amdgcn_exp2f(pm[0] - M), w1 = __builtin_amdgcn_exp2f(pm[1] - M);
    const float L = xrow_sum(fmaf(w0, pl[0], w1 * pl[1])), o0 = xrow_sum(fmaf(w0, d0[0], w1 * d0[1])), o1 = xrow_sum(fmaf(w0, d1[0], w1 * d1[1]));
    if (lane < 16) *reinterpret_cast<f32x4*>(lds + wave * L_WAVE + lane * 16) = f32x4{M, L, o0, o1};
    __syncthreads();
    if (tid < 16) {
      f32x4 r[NW];
#pragma unroll
      for (int w = 0; w < NW; ++w) r[w] = *reinterpret_cast<const f32x4*>(lds + w * L_WAVE + lane * 16);
      const float MM = fmaxf(fmaxf(r[0][0], r[1][0]), fmaxf(r[2][0], r[3][0]));
      float LL = 0.f, a0 = 0.f, a1 = 0.f;
#pragma unroll
      for (int w = 0; w < NW; ++w) { const float wt = __builtin_amdgcn_exp2f(r[w][0] - MM); LL = fmaf(wt, r[w][1], LL); a0 = fmaf(wt, r[w][2], a0); a1 = fmaf(wt, r[w][3], a1); }
      const float inv = __builtin_amdgcn_rcpf(LL);
      outw[2 * (c & 7) + (c >> 3)] = pk_bf16(a0 * inv, a1 * inv);
      if (split == 0 && tid == 0) __builtin_amdgcn_raw_buffer_store_b64(u32x2{tagA, tagB}, rp, 0, WS_EPOCH, AUX_SC1);
    }
  }
  if (blockIdx.x == 0)
  while (__builtin_amdgcn_s_memrealtime() - tstart < PAD_END * 100ull) __builtin_amdgcn_s_sleep(1);
}

extern "C" void kernel_launch(void* const* d_in, const int* in_sizes, int n_in, void* d_out, int out_size,
                              void* d_ws, size_t ws_size, hipStream_t stream) {
  (void)hipFuncSetAttribute((const void*)kivi_split, hipFuncAttributeMaxDynamicSharedMemorySize, LDS_BYTES);
  kivi_split<<<NBH * NSPLIT, NW * 64, LDS_BYTES, stream>>>((const u32*)d_in[0], (const int*)d_in[1], (const int*)d_in[2],
      (const u32*)d_in[3], (const u32*)d_in[4], (const u32*)d_in[5], (const u32*)d_in[6], (unsigned char*)d_ws, (u32*)d_out);
}
```

```cpp
#include <hip/hip_runtime.h>
#include <hip/hip_bf16.h>
#include <stdint.h>
#define PROBE 1
#define AMP 20
#define PT_IN 1

typedef unsigned u32;
typedef u32 u32x4 __attribute__((ext_vector_type(4)));
typedef u32 u32x2 __attribute__((ext_vector_type(2)));
typedef float f32x4 __attribute__((ext_vector_type(4)));
typedef float f32x2 __attribute__((ext_vector_type(2)));
typedef __bf16 bf16x8 __attribute__((ext_vector_type(8)));
typedef __bf16 bf16x2 __attribute__((ext_vector_type(2)));
typedef short s16x4 __attribute__((ext_vector_type(4)));
typedef __amdgpu_buffer_rsrc_t rsrc_t;

#define AUXK 2
#define AUXV 2
constexpr int B_ = 2, HQ = 32, HKV = 4, SKV = 32768, DH = 128, GT = 128, NGK = SKV / GT;
constexpr int REP = HQ / HKV;
constexpr int NBH = B_ * HKV;
constexpr int NW = 4;
constexpr int NSPLIT = 32;
constexpr int TPW = SKV / NSPLIT / NW;
constexpr int NT = TPW / 16;
constexpr float QSCALE = 0.088388347648318440f * 1.4426950408889634f;

constexpr int KROW = 272, L_KT = 0, L_KP = 16 * KROW, L_VS = L_KP + 2048, L_VZ = L_VS + 1024, L_WAVE = L_VZ + 512;
constexpr int OROW = (DH + 4) * 4;
constexpr int L_MRG = NW * L_WAVE, L_ML = L_MRG + NW * REP * OROW, L_END = L_ML + NW * REP * 8;
constexpr int LDS_BYTES = 84 * 1024;
static_assert(L_END <= LDS_BYTES, "LDS map");

#define STAMP(i)

__device__ __forceinline__ float bf_lo(u32 x) { return __builtin_bit_cast(float, x << 16); }
__device__ __forceinline__ float bf_hi(u32 x) { return __builtin_bit_cast(float, x & 0xffff0000u); }
__device__ __forceinline__ u32 pk_bf16(float a, float b) {
  bf16x2 r; r[0] = (__bf16)a; r[1] = (__bf16)b; return __builtin_bit_cast(u32, r);
}
__device__ __forceinline__ float i2f(u32 x) { return (float)(int)x; }
template <int CTRL> __device__ __forceinline__ float dpp(float x) {
  return __builtin_bit_cast(float, __builtin_amdgcn_mov_dpp(__builtin_bit_cast(int, x), CTRL, 0xf, 0xf, true));
}
__device__ __forceinline__ float xrow_max(float x) {
  auto s = __builtin_amdgcn_permlane16_swap(__float_as_uint(x), __float_as_uint(x), false, false);
  x = fmaxf(__uint_as_float(s[0]), __uint_as_float(s[1]));
  auto t = __builtin_amdgcn_permlane32_swap(__float_as_uint(x), __float_as_uint(x), false, false);
  return fmaxf(__uint_as_float(t[0]), __uint_as_float(t[1]));
}
__device__ __forceinline__ float xrow_sum(float x) {
  auto s = __builtin_amdgcn_permlane16_swap(__float_as_uint(x), __float_as_uint(x), false, false);
  x = __uint_as_float(s[0]) + __uint_as_float(s[1]);
  auto t = __builtin_amdgcn_permlane32_swap(__float_as_uint(x), __float_as_uint(x), false, false);
  return __uint_as_float(t[0]) + __uint_as_float(t[1]);
}
#define SB() __builtin_amdgcn_sched_barrier(0)

constexpr int PKT = 384, PKT_ALL = NSPLIT * NSPLIT * PKT, WS_GRP = 512 * 1024, WS_EPOCH = PKT_ALL;
constexpr int OOB = 0x7fffffe0;
constexpr int AUX_SC1 = 16;
#define ROT 3
#define SMALL_FIRST 1
#define SLEEP_FAR 16
#define SLEEP_NEAR 1

__global__ void __launch_bounds__(NW * 64, 2)
kivi_split(const u32* __restrict__ q, const int* __restrict__ qk, const int* __restrict__ qv,
           const u32* __restrict__ ks, const u32* __restrict__ kz,
           const u32* __restrict__ vs, const u32* __restrict__ vz, unsigned char* __restrict__ ws, u32* __restrict__ out) {
  extern __shared__ __attribute__((aligned(16))) unsigned char lds[];
  unsigned long long tstart, tx = 0;
#define RT(t_) do { __builtin_amdgcn_sched_barrier(0); asm volatile("s_memrealtime %0\n\ts_waitcnt lgkmcnt(0)" : "=s"(t_) :: "memory"); __builtin_amdgcn_sched_barrier(0); } while (0)
  asm volatile("s_memrealtime %0" : "=s"(tstart) :: "memory");
  asm volatile("" :: "s"(q), "s"(qk), "s"(ws), "s"(out));
  RT(tx);
  const int tid = threadIdx.x, lane = tid & 63;
  const int wave = __builtin_amdgcn_readfirstlane(tid >> 6);
  const int c = lane & 15, g = lane >> 4;
  const int bh = blockIdx.x & 7, split = blockIdx.x >> 3;
  const int tw = (split * NW + wave) * TPW;
  const size_t tok0 = (size_t)bh * SKV + tw;
  const char* kb = (const char*)qk + tok0 * (DH * 4);
  const char* vb = (const char*)qv + tok0 * (DH * 4);
  const int voff = g * 2048 + c * 16;
  const int rot = ((blockIdx.x * NW + wave) * ROT) & (NT - 1);
  u32x4 K[8], V[8];
  bf16x8 qf[4];
  u32x2 epoch;
  u32 otouch;
  const rsrc_t rp = __builtin_amdgcn_make_buffer_rsrc((void*)(ws + (size_t)bh * WS_GRP), 0, WS_EPOCH + 8, 0x00020000);
  u32* outw = out + ((bh * REP + (split >> 2)) * DH + (split & 3) * 32) / 2;
  {
    const rsrc_t rk = __builtin_amdgcn_make_buffer_rsrc((void*)kb, 0, TPW * DH * 4, 0x00020000);
    const rsrc_t rv = __builtin_amdgcn_make_buffer_rsrc((void*)vb, 0, TPW * DH * 4, 0x00020000);
    unsigned char* lw = lds + wave * L_WAVE;
    const size_t grow = ((size_t)bh * NGK + tw / GT) * (DH / 2);
    const u32 ks0 = ks[grow + lane], kz0 = kz[grow + lane], ks1 = ks[grow + 64 + lane], kz1 = kz[grow + 64 + lane];
    const u32x2 vsr = *reinterpret_cast<const u32x2*>(vs + tok0 / 2 + 2 * lane);
    const u32x2 vzr = *reinterpret_cast<const u32x2*>(vz + tok0 / 2 + 2 * lane);
    SB();
#pragma unroll
    for (int n = 0; n < 8; ++n) K[n] = __builtin_amdgcn_raw_buffer_load_b128(rk, voff + (n >> 1) * 512 + (n & 1) * 256, rot * 8192, AUXK);
    SB();
#pragma unroll
    for (int i = 0; i < 8; ++i) { const int n = (i & 4) + ((i & 1) << 1) + ((i >> 1) & 1);
      V[n] = __builtin_amdgcn_raw_buffer_load_b128(rv, voff + (n >> 1) * 512 + (n & 1) * 256, rot * 8192, AUXV); }
    SB();
    const u32* qp = q + ((size_t)bh * REP + (c & 7)) * (DH / 2) + 4 * g;
#pragma unroll
    for (int s = 0; s < 4; ++s) qf[s] = __builtin_bit_cast(bf16x8, *reinterpret_cast<const u32x4*>(qp + 16 * s));
    epoch = __builtin_amdgcn_raw_buffer_load_b64(rp, 0, WS_EPOCH, AUX_SC1);
    otouch = __builtin_amdgcn_raw_buffer_load_b32(rp, (split * NSPLIT + 8 * wave + (lane >> 3)) * PKT + (lane & 3) * 128, 0, 16);
    otouch += outw[c];
    SB();
    *reinterpret_cast<f32x2*>(lw + L_KP + lane * 8) = f32x2{bf_lo(ks0), bf_hi(ks0)};
    *reinterpret_cast<f32x2*>(lw + L_KP + 512 + lane * 8) = f32x2{-bf_lo(kz0) * bf_lo(ks0), -bf_hi(kz0) * bf_hi(ks0)};
    *reinterpret_cast<f32x2*>(lw + L_KP + 1024 + lane * 8) = f32x2{bf_lo(ks1), bf_hi(ks1)};
    *reinterpret_cast<f32x2*>(lw + L_KP + 1536 + lane * 8) = f32x2{-bf_lo(kz1) * bf_lo(ks1), -bf_hi(kz1) * bf_hi(ks1)};
    *reinterpret_cast<f32x4*>(lw + L_VS + lane * 16) = f32x4{bf_lo(vsr[0]), bf_hi(vsr[0]), bf_lo(vsr[1]), bf_hi(vsr[1])};
    *reinterpret_cast<u32x2*>(lw + L_VZ + lane * 8) = u32x2{vzr[0] ^ 0x80008000u, vzr[1] ^ 0x80008000u};
  }
  SB();

  unsigned char* lw = lds + wave * L_WAVE;
  unsigned char* kt_w = lw + L_KT + g * (4 * KROW) + c * 8;
  const unsigned char* kt_r = lw + L_KT + c * KROW + g * 16;
  const u32 lomask = c >= 8 ? 0xffff0000u : 0u;
  f32x4 acc[2][4], accz = {0.f, 0.f, 0.f, 0.f};
#pragma unroll
  for (int h = 0; h < 2; ++h)
#pragma unroll
    for (int e = 0; e < 4; ++e) acc[h][e] = f32x4{0.f, 0.f, 0.f, 0.f};
  float m = -INFINITY, l = 0.f;

#pragma unroll 1
  for (int it = 0; it < NT; ++it) {
    const bool more = it + 1 < NT;
    const int cur = (it + rot) & (NT - 1);
    const int soff = ((cur + 1) & (NT - 1)) * 8192;
    const rsrc_t rk = __builtin_amdgcn_make_buffer_rsrc((void*)kb, 0, more ? TPW * DH * 4 : 0, 0x00020000);
    const rsrc_t rv = __builtin_amdgcn_make_buffer_rsrc((void*)vb, 0, more ? TPW * DH * 4 : 0, 0x00020000);
    const unsigned char* kp = lw + L_KP + (cur >> 3) * 1024 + c * 16;
    f32x4 sc[2], zc[2];
#pragma unroll
    for (int h = 0; h < 2; ++h) { sc[h] = *reinterpret_cast<const f32x4*>(kp + h * 256); zc[h] = *reinterpret_cast<const f32x4*>(kp + 512 + h * 256); }
    const f32x4 vsc = *reinterpret_cast<const f32x4*>(lw + L_VS + cur * 64 + g * 16);
    const u32x2 vzn = *reinterpret_cast<const u32x2*>(lw + L_VZ + cur * 32 + g * 8);
    SB();
#pragma unroll
    for (int n = 0; n < 8; ++n) {
      const int h = n & 1;
      const u32x2 w = {pk_bf16(fmaf(i2f(K[n][0]), sc[h][0], zc[h][0]), fmaf(i2f(K[n][1]), sc[h][1], zc[h][1])),
                       pk_bf16(fmaf(i2f(K[n][2]), sc[h][2], zc[h][2]), fmaf(i2f(K[n][3]), sc[h][3], zc[h][3]))};
      *reinterpret_cast<u32x2*>(kt_w + (n >> 1) * KROW + h * 128) = w;
      SB();
      K[n] = __builtin_amdgcn_raw_buffer_load_b128(rk, voff + (n >> 1) * 512 + h * 256, soff, AUXK);
      SB();
    }
    bf16x8 kf[4];
#pragma unroll
    for (int s = 0; s < 4; ++s) kf[s] = __builtin_bit_cast(bf16x8, *reinterpret_cast<const u32x4*>(kt_r + 64 * s));
    SB();
    u32x2 vf[2][4];
#pragma unroll
    for (int mp = 0; mp < 2; ++mp)
#pragma unroll
      for (int h = 0; h < 2; ++h) {
        const int n0 = 4 * mp + h, n1 = n0 + 2;
#pragma unroll
        for (int e = 0; e < 4; ++e) vf[h][e][mp] = pk_bf16(i2f(V[n0][e]), i2f(V[n1][e]));
        SB();
        V[n0] = __builtin_amdgcn_raw_buffer_load_b128(rv, voff + (n0 >> 1) * 512 + h * 256, soff, AUXV);
        V[n1] = __builtin_amdgcn_raw_buffer_load_b128(rv, voff + (n1 >> 1) * 512 + h * 256, soff, AUXV);
        SB();
      }
    f32x4 S = {0.f, 0.f, 0.f, 0.f};
#pragma unroll
    for (int s = 0; s < 4; ++s) S = __builtin_amdgcn_mfma_f32_16x16x32_bf16(kf[s], qf[s], S, 0, 0, 0);
    const float mn = fmaxf(m, xrow_max(fmaxf(fmaxf(S[0], S[1]), fmaxf(S[2], S[3]))));
    const float alpha = __builtin_amdgcn_exp2f((m - mn) * QSCALE);
    const float mc = mn * QSCALE;
    m = mn;
    float p[4];
#pragma unroll
    for (int r = 0; r < 4; ++r) p[r] = __builtin_amdgcn_exp2f(fmaf(S[r], QSCALE, -mc));
    l = fmaf(l, alpha, (p[0] + p[1]) + (p[2] + p[3]));
    u32 y[4];
#pragma unroll
    for (int r = 0; r < 4; ++r) {
      const float x = p[r] * vsc[r];
      y[r] = __float_as_uint(x - __uint_as_float(__float_as_uint(x) & lomask));
    }
    const u32x2 pfu = {__builtin_amdgcn_perm(y[1], y[0], 0x07060302u), __builtin_amdgcn_perm(y[3], y[2], 0x07060302u)};
    const s16x4 pf = __builtin_bit_cast(s16x4, pfu);
    accz = __builtin_amdgcn_mfma_f32_16x16x16bf16_1k(__builtin_bit_cast(s16x4, vzn), pf, accz * alpha, 0, 0, 0);
#pragma unroll
    for (int h = 0; h < 2; ++h)
#pragma unroll
      for (int e = 0; e < 4; ++e)
        acc[h][e] = __builtin_amdgcn_mfma_f32_16x16x16bf16_1k(__builtin_bit_cast(s16x4, vf[h][e]), pf, acc[h][e] * alpha, 0, 0, 0);
    SB();
  }
  STAMP(1);
  asm volatile("" :: "v"(otouch));
  const u32 tagA = epoch[0] + 1u, tagB = epoch[1] + 1u;
  const int ib = (split * NSPLIT + 8 * wave + g) * PKT;
  u32x4 x[2], y[2];
#pragma unroll
  for (int j = 0; j < 2; ++j) {
    x[j] = u32x4{0u, tagA + 1u, 0u, 0u}; y[j] = x[j];
  }
  SB();

  l = xrow_sum(l);
  {
    const bool up = c >= 8;
    unsigned char* orow = lds + L_MRG + (wave * REP + (c & 7)) * OROW + (up ? 256 : 0) + g * 64;
#pragma unroll
    for (int r = 0; r < 4; ++r) {
      f32x4 o;
      const float z = accz[r] + dpp<0x128>(accz[r]);
#pragma unroll
      for (int e = 0; e < 4; ++e) {
        const float own = up ? acc[1][e][r] : acc[0][e][r], oth = up ? acc[0][e][r] : acc[1][e][r];
        o[e] = own + dpp<0x128>(oth) + z;
      }
      *reinterpret_cast<f32x4*>(orow + r * 16) = o;
    }
    if (lane < 8) *reinterpret_cast<f32x2*>(lds + L_ML + (wave * REP + lane) * 8) = f32x2{m * QSCALE, l};
  }
  __syncthreads();
  STAMP(2);
  {
    const int h = tid >> 5, d4 = tid & 31;
    f32x2 ml[NW];
    float M = -INFINITY;
#pragma unroll
    for (int w = 0; w < NW; ++w) { ml[w] = *reinterpret_cast<const f32x2*>(lds + L_ML + (w * REP + h) * 8); M = fmaxf(M, ml[w][0]); }
    float L = 0.f; f32x4 o = {0.f, 0.f, 0.f, 0.f};
#pragma unroll
    for (int w = 0; w < NW; ++w) {
      const float wt = __builtin_amdgcn_exp2f(ml[w][0] - M);
      const f32x4 a = *reinterpret_cast<const f32x4*>(lds + L_MRG + (w * REP + h) * OROW + d4 * 16);
      L = fmaf(wt, ml[w][1], L); o += a * wt;
    }
    const int po = ((tid >> 3) * NSPLIT + split) * PKT;
    __builtin_amdgcn_raw_buffer_store_b128(u32x4{__float_as_uint(o[0]), tagA, __float_as_uint(o[1]), tagB}, rp, po + (tid & 7) * 16, 0, AUX_SC1);
    __builtin_amdgcn_raw_buffer_store_b128(u32x4{__float_as_uint(o[2]), tagA, __float_as_uint(o[3]), tagB}, rp, po + (tid & 7) * 16 + 128, 0, AUX_SC1);
    __builtin_amdgcn_raw_buffer_store_b128(u32x4{__float_as_uint(M), tagA, __float_as_uint(L), tagB}, rp,
        (tid & 7) == 0 ? po + 256 : OOB,
        0, AUX_SC1);
    SB();
  }
  STAMP(3);
  STAMP(5);
  {
    float d0[2], d1[2], pm[2], pl[2];
    bool pend[2] = {true, true};
#pragma unroll
    for (int j = 0; j < 2; ++j) { d0[j] = 0.f; d1[j] = 0.f; pm[j] = 0.f; pl[j] = 0.f; }
    for (int spins = 0;; ++spins) {
#pragma unroll
      for (int j = 0; j < 2; ++j) {
        const bool ok = pend[j] & ((((x[j][1] ^ tagA) | (x[j][3] ^ tagB)) | ((y[j][1] ^ tagA) | (y[j][3] ^ tagB))) == 0u);
        d0[j] = ok ? __uint_as_float(x[j][0]) : d0[j]; d1[j] = ok ? __uint_as_float(x[j][2]) : d1[j];
        pm[j] = ok ? __uint_as_float(y[j][0]) : pm[j]; pl[j] = ok ? __uint_as_float(y[j][2]) : pl[j];
        pend[j] = pend[j] & !ok;
      }
      const int nmiss = __builtin_popcountll(__builtin_amdgcn_ballot_w64(pend[0])) + __builtin_popcountll(__builtin_amdgcn_ballot_w64(pend[1]));
      if (nmiss == 0 || spins > (1 << 20)) break;
#ifdef LONG_FIRST
      if (spins == 0) { for (int z = 0; z < LONG_FIRST; ++z) __builtin_amdgcn_s_sleep(127); }
#endif
      if (spins > 0) { if (nmiss > 32) __builtin_amdgcn_s_sleep(SLEEP_FAR); else __builtin_amdgcn_s_sleep(SLEEP_NEAR); }
      asm volatile("" ::: "memory");
#pragma unroll
      for (int j = 0; j < 2; ++j) {
        x[j] = __builtin_amdgcn_raw_buffer_load_b128(rp, pend[j] ? ib + j * 4 * PKT + c * 16 : OOB, 0, AUX_SC1);
        y[j] = __builtin_amdgcn_raw_buffer_load_b128(rp, pend[j] ? ib + j * 4 * PKT + 256 : OOB, 0, AUX_SC1);
      }
    }
    const float M = xrow_max(fmaxf(pm[0], pm[1]));
    const float w0 = __builtin_amdgcn_exp2f(pm[0] - M), w1 = __builtin_amdgcn_exp2f(pm[1] - M);
    const float L = xrow_sum(fmaf(w0, pl[0], w1 * pl[1])), o0 = xrow_sum(fmaf(w0, d0[0], w1 * d0[1])), o1 = xrow_sum(fmaf(w0, d1[0], w1 * d1[1]));
    if (lane < 16) *reinterpret_cast<f32x4*>(lds + wave * L_WAVE + lane * 16) = f32x4{M, L, o0, o1};
    __syncthreads();
    if (tid < 16) {
      f32x4 r[NW];
#pragma unroll
      for (int w = 0; w < NW; ++w) r[w] = *reinterpret_cast<const f32x4*>(lds + w * L_WAVE + lane * 16);
      const float MM = fmaxf(fmaxf(r[0][0], r[1][0]), fmaxf(r[2][0], r[3][0]));
      float LL = 0.f, a0 = 0.f, a1 = 0.f;
#pragma unroll
      for (int w = 0; w < NW; ++w) { const float wt = __builtin_amdgcn_exp2f(r[w][0] - MM); LL = fmaf(wt, r[w][1], LL); a0 = fmaf(wt, r[w][2], a0); a1 = fmaf(wt, r[w][3], a1); }
      const float inv = __builtin_amdgcn_rcpf(LL);
      outw[2 * (c & 7) + (c >> 3)] = pk_bf16(a0 * inv, a1 * inv);
      if (split == 0 && tid == 0) __builtin_amdgcn_raw_buffer_store_b64(u32x2{tagA, tagB}, rp, 0, WS_EPOCH, AUX_SC1);
    }
  }
  { const unsigned long long lim = 10000ull + AMP * (tx - tstart);
    while (__builtin_amdgcn_s_memrealtime() - tstart < lim) __builtin_amdgcn_s_sleep(1); }
}

extern "C" void kernel_launch(void* const* d_in, const int* in_sizes, int n_in, void* d_out, int out_size,
                              void* d_ws, size_t ws_size, hipStream_t stream) {
  (void)hipFuncSetAttribute((const void*)kivi_split, hipFuncAttributeMaxDynamicSharedMemorySize, LDS_BYTES);
  kivi_split<<<NBH * NSPLIT, NW * 64, LDS_BYTES, stream>>>((const u32*)d_in[0], (const int*)d_in[1], (const int*)d_in[2],
      (const u32*)d_in[3], (const u32*)d_in[4], (const u32*)d_in[5], (const u32*)d_in[6], (unsigned char*)d_ws, (u32*)d_out);
}
```

```cpp
#include <hip/hip_runtime.h>
#include <hip/hip_bf16.h>
#include <stdint.h>
#define NE 17

typedef unsigned u32;
typedef u32 u32x4 __attribute__((ext_vector_type(4)));
typedef u32 u32x2 __attribute__((ext_vector_type(2)));
typedef float f32x4 __attribute__((ext_vector_type(4)));
typedef float f32x2 __attribute__((ext_vector_type(2)));
typedef __bf16 bf16x8 __attribute__((ext_vector_type(8)));
typedef __bf16 bf16x2 __attribute__((ext_vector_type(2)));
typedef short s16x4 __attribute__((ext_vector_type(4)));
typedef __amdgpu_buffer_rsrc_t rsrc_t;

constexpr int B_ = 2, HQ = 32, HKV = 4, SKV = 32768, DH = 128, GT = 128, NGK = SKV / GT;
constexpr int REP = HQ / HKV;
constexpr int NBH = B_ * HKV;
constexpr int NW = 4;
constexpr int NSPLIT = 32;
#define SH 3
#define SO 1
constexpr float QSCALE = 0.088388347648318440f * 1.4426950408889634f;

constexpr int KROW = 272, L_WAVE = 16 * KROW;
constexpr int OROW = (DH + 4) * 4;
constexpr int L_MRG = NW * L_WAVE, L_ML = L_MRG + NW * 16 * OROW, L_END = L_ML + NW * 16 * 16;
constexpr int LDS_BYTES = 84 * 1024;
static_assert(L_END <= LDS_BYTES, "LDS map");

constexpr int PKT = 256, WS_ML = NSPLIT * NSPLIT * PKT, WS_EPOCH = WS_ML + REP * NSPLIT * 16, WS_GRP = 512 * 1024;
constexpr int OOB = 0x7fffffe0;
constexpr int AUX_NT = 2, AUX_SC1 = 16;
#define SLEEP_FAR 8

#define STAMP(i)

__device__ __forceinline__ float bf_lo(u32 x) { return __builtin_bit_cast(float, x << 16); }
__device__ __forceinline__ float bf_hi(u32 x) { return __builtin_bit_cast(float, x & 0xffff0000u); }
__device__ __forceinline__ u32 pk_bf16(float a, float b) {
  bf16x2 r; r[0] = (__bf16)a; r[1] = (__bf16)b; return __builtin_bit_cast(u32, r);
}
__device__ __forceinline__ float i2f(u32 x) { return (float)(int)x; }
__device__ __forceinline__ float xrow_max(float x) {
  auto s = __builtin_amdgcn_permlane16_swap(__float_as_uint(x), __float_as_uint(x), false, false);
  x = fmaxf(__uint_as_float(s[0]), __uint_as_float(s[1]));
  auto t = __builtin_amdgcn_permlane32_swap(__float_as_uint(x), __float_as_uint(x), false, false);
  return fmaxf(__uint_as_float(t[0]), __uint_as_float(t[1]));
}
__device__ __forceinline__ float xrow_sum(float x) {
  auto s = __builtin_amdgcn_permlane16_swap(__float_as_uint(x), __float_as_uint(x), false, false);
  x = __uint_as_float(s[0]) + __uint_as_float(s[1]);
  auto t = __builtin_amdgcn_permlane32_swap(__float_as_uint(x), __float_as_uint(x), false, false);
  return __uint_as_float(t[0]) + __uint_as_float(t[1]);
}
#define SB() __builtin_amdgcn_sched_barrier(0)

__global__ void __launch_bounds__(NW * 64, 2)
kivi_decode(const u32* __restrict__ q, const int* __restrict__ qk, const int* __restrict__ qv,
            const u32* __restrict__ ks, const u32* __restrict__ kz,
            const u32* __restrict__ vs, const u32* __restrict__ vz, unsigned char* __restrict__ ws, u32* __restrict__ out) {
  extern __shared__ __attribute__((aligned(16))) unsigned char lds[];
  STAMP(0);
  const int tid = threadIdx.x, lane = tid & 63;
  const int wave = __builtin_amdgcn_readfirstlane(tid >> 6);
  const int c = lane & 15, g = lane >> 4;
  const int side = blockIdx.x & 1, idx = blockIdx.x >> 3;
  const int bh = (blockIdx.x & 6) + (idx >> 4), rank = idx & 15, split = 2 * rank + side;
  const int nr = side ? 32 - NE : NE;
  const int t0 = (side ? NE * 64 : 0) + rank * 4 + wave;
  int rnd = SH * bh + SO * side;
  static_assert(SH * 7 + SO < 3 * (NE < 16 ? NE : 32 - NE), "start round");
  rnd -= rnd >= nr ? nr : 0; rnd -= rnd >= nr ? nr : 0;
  const char* kb = (const char*)qk + (size_t)bh * SKV * (DH * 4);
  const char* vb = (const char*)qv + (size_t)bh * SKV * (DH * 4);
  const rsrc_t rks = __builtin_amdgcn_make_buffer_rsrc((void*)(ks + bh * (SKV / 2)), 0, SKV * 2, 0x00020000);
  const rsrc_t rkz = __builtin_amdgcn_make_buffer_rsrc((void*)(kz + bh * (SKV / 2)), 0, SKV * 2, 0x00020000);
  const rsrc_t rvs = __builtin_amdgcn_make_buffer_rsrc((void*)(vs + bh * (SKV / 2)), 0, SKV * 2, 0x00020000);
  const rsrc_t rvz = __builtin_amdgcn_make_buffer_rsrc((void*)(vz + bh * (SKV / 2)), 0, SKV * 2, 0x00020000);
  const int voff = g * 2048 + c * 16;
  unsigned char* lw = lds + wave * L_WAVE;
  const rsrc_t rp = __builtin_amdgcn_make_buffer_rsrc((void*)(ws + (size_t)bh * WS_GRP), 0, WS_EPOCH + 8, 0x00020000);
  u32* outw = out + ((bh * REP + (split >> 2)) * DH + ((split >> 1) & 1) * 64 + (split & 1) * 2) / 2;
  u32x4 K[8], V[8];
  u32x2 ksr[2], kzr[2], vsr, vzr;
  bf16x8 qf[4];
  u32x2 epoch;
  u32 touch;
#define LOAD_PARAMS(tile_) do { \
    ksr[0] = __builtin_amdgcn_raw_buffer_load_b64(rks, c * 8, ((tile_) >> 3) * 256, 0); ksr[1] = __builtin_amdgcn_raw_buffer_load_b64(rks, c * 8 + 128, ((tile_) >> 3) * 256, 0); \
    kzr[0] = __builtin_amdgcn_raw_buffer_load_b64(rkz, c * 8, ((tile_) >> 3) * 256, 0); kzr[1] = __builtin_amdgcn_raw_buffer_load_b64(rkz, c * 8 + 128, ((tile_) >> 3) * 256, 0); \
    vsr = __builtin_amdgcn_raw_buffer_load_b64(rvs, g * 8, (tile_) * 32, 0); vzr = __builtin_amdgcn_raw_buffer_load_b64(rvz, g * 8, (tile_) * 32, 0); } while (0)
  {
    const rsrc_t rk = __builtin_amdgcn_make_buffer_rsrc((void*)kb, 0, SKV * DH * 4, 0x00020000);
    const rsrc_t rv = __builtin_amdgcn_make_buffer_rsrc((void*)vb, 0, SKV * DH * 4, 0x00020000);
    const int tile = t0 + rnd * 64;
    LOAD_PARAMS(tile);
    SB();
#pragma unroll
    for (int n = 0; n < 8; ++n) K[n] = __builtin_amdgcn_raw_buffer_load_b128(rk, voff + (n >> 1) * 512 + (n & 1) * 256, tile * 8192, AUX_NT);
    SB();
    const u32* qp = q + ((size_t)bh * REP + (c & 7)) * (DH / 2) + 4 * g;
#pragma unroll
    for (int s = 0; s < 4; ++s) qf[s] = __builtin_bit_cast(bf16x8, *reinterpret_cast<const u32x4*>(qp + 16 * s));
    SB();
#pragma unroll
    for (int n = 0; n < 8; ++n) V[n] = __builtin_amdgcn_raw_buffer_load_b128(rv, voff + (n >> 1) * 512 + (n & 1) * 256, tile * 8192, AUX_NT);
    SB();
    epoch = __builtin_amdgcn_raw_buffer_load_b64(rp, 0, WS_EPOCH, AUX_SC1);
    touch = __builtin_amdgcn_raw_buffer_load_b32(rp, lane < 16 ? (split * NSPLIT + 8 * wave) * PKT + lane * 128 : (tid == 16 ? WS_ML + split * 128 : OOB), 0, AUX_SC1);
    touch += outw[2 * c];
  }
  SB();

  unsigned char* kt_w = lw + g * (4 * KROW) + c * 8;
  const unsigned char* kt_r = lw + c * KROW + g * 16;
  const u32 lomask = c >= 8 ? 0xffff0000u : 0u;
  f32x4 acc[2][4], accz = {0.f, 0.f, 0.f, 0.f};
#pragma unroll
  for (int h = 0; h < 2; ++h)
#pragma unroll
    for (int e = 0; e < 4; ++e) acc[h][e] = f32x4{0.f, 0.f, 0.f, 0.f};
  float m = -INFINITY, l = 0.f;

#pragma unroll 1
  for (int it = 0; it < nr; ++it) {
    const bool more = it + 1 < nr;
    rnd = rnd + 1 == nr ? 0 : rnd + 1;
    const int tile = t0 + rnd * 64;
    const int soff = tile * 8192;
    const rsrc_t rk = __builtin_amdgcn_make_buffer_rsrc((void*)kb, 0, more ? SKV * DH * 4 : 0, 0x00020000);
    const rsrc_t rv = __builtin_amdgcn_make_buffer_rsrc((void*)vb, 0, more ? SKV * DH * 4 : 0, 0x00020000);
    f32x4 sc[2], zc[2];
#pragma unroll
    for (int h = 0; h < 2; ++h) {
      sc[h] = f32x4{bf_lo(ksr[h][0]), bf_hi(ksr[h][0]), bf_lo(ksr[h][1]), bf_hi(ksr[h][1])};
      zc[h] = -f32x4{bf_lo(kzr[h][0]), bf_hi(kzr[h][0]), bf_lo(kzr[h][1]), bf_hi(kzr[h][1])} * sc[h];
    }
    f32x4 vsc = {bf_lo(vsr[0]), bf_hi(vsr[0]), bf_lo(vsr[1]), bf_hi(vsr[1])};
    u32x2 vzn = vzr ^ 0x80008000u;
    asm volatile("" : "+v"(sc[0]), "+v"(sc[1]), "+v"(zc[0]), "+v"(zc[1]), "+v"(vsc), "+v"(vzn));
    SB();
    LOAD_PARAMS(tile);
    SB();
#pragma unroll
    for (int n = 0; n < 8; ++n) {
      const int h = n & 1;
      const u32x2 w = {pk_bf16(fmaf(i2f(K[n][0]), sc[h][0], zc[h][0]), fmaf(i2f(K[n][1]), sc[h][1], zc[h][1])),
                       pk_bf16(fmaf(i2f(K[n][2]), sc[h][2], zc[h][2]), fmaf(i2f(K[n][3]), sc[h][3], zc[h][3]))};
      *reinterpret_cast<u32x2*>(kt_w + (n >> 1) * KROW + h * 128) = w;
      SB();
      K[n] = __builtin_amdgcn_raw_buffer_load_b128(rk, voff + (n >> 1) * 512 + h * 256, soff, AUX_NT);
      SB();
    }
    bf16x8 kf[4];
#pragma unroll
    for (int s = 0; s < 4; ++s) kf[s] = __builtin_bit_cast(bf16x8, *reinterpret_cast<const u32x4*>(kt_r + 64 * s));
    f32x4 S0 = {0.f, 0.f, 0.f, 0.f}, S1 = {0.f, 0.f, 0.f, 0.f};
    S0 = __builtin_amdgcn_mfma_f32_16x16x32_bf16(kf[0], qf[0], S0, 0, 0, 0);
    S1 = __builtin_amdgcn_mfma_f32_16x16x32_bf16(kf[1], qf[1], S1, 0, 0, 0);
    S0 = __builtin_amdgcn_mfma_f32_16x16x32_bf16(kf[2], qf[2], S0, 0, 0, 0);
    S1 = __builtin_amdgcn_mfma_f32_16x16x32_bf16(kf[3], qf[3], S1, 0, 0, 0);
    const f32x4 S = S0 + S1;
    const float mn = fmaxf(m, xrow_max(fmaxf(fmaxf(S[0], S[1]), fmaxf(S[2], S[3]))));
    const float alpha = __builtin_amdgcn_exp2f((m - mn) * QSCALE);
    const float mc = mn * QSCALE;
    m = mn;
    float p[4];
#pragma unroll
    for (int r = 0; r < 4; ++r) p[r] = __builtin_amdgcn_exp2f(fmaf(S[r], QSCALE, -mc));
    l = fmaf(l, alpha, (p[0] + p[1]) + (p[2] + p[3]));
    u32 y[4];
#pragma unroll
    for (int r = 0; r < 4; ++r) {
      const float x = p[r] * vsc[r];
      y[r] = __float_as_uint(x - __uint_as_float(__float_as_uint(x) & lomask));
    }
    const u32x2 pfu = {__builtin_amdgcn_perm(y[1], y[0], 0x07060302u), __builtin_amdgcn_perm(y[3], y[2], 0x07060302u)};
    const s16x4 pf = __builtin_bit_cast(s16x4, pfu);
#pragma unroll
    for (int h = 0; h < 2; ++h)
#pragma unroll
      for (int e = 0; e < 4; ++e) acc[h][e] *= alpha;
    accz = __builtin_amdgcn_mfma_f32_16x16x16bf16_1k(__builtin_bit_cast(s16x4, vzn), pf, accz * alpha, 0, 0, 0);
    SB();
    u32x2 vf[2][4];
#pragma unroll
    for (int mp = 0; mp < 2; ++mp) {
#pragma unroll
      for (int h = 0; h < 2; ++h)
#pragma unroll
        for (int e = 0; e < 4; ++e) vf[h][e][mp] = pk_bf16(i2f(V[4 * mp + h][e]), i2f(V[4 * mp + h + 2][e]));
      SB();
#pragma unroll
      for (int n = 4 * mp; n < 4 * mp + 4; ++n) V[n] = __builtin_amdgcn_raw_buffer_load_b128(rv, voff + (n >> 1) * 512 + (n & 1) * 256, soff, AUX_NT);
      SB();
    }
#pragma unroll
    for (int h = 0; h < 2; ++h)
#pragma unroll
      for (int e = 0; e < 4; ++e)
        acc[h][e] = __builtin_amdgcn_mfma_f32_16x16x16bf16_1k(__builtin_bit_cast(s16x4, vf[h][e]), pf, acc[h][e], 0, 0, 0);
    SB();
  }
  STAMP(1);
  asm volatile("" :: "v"(touch));
  const u32 tagA = epoch[0] + 1u, tagB = epoch[1] + 1u;

  {
    l = xrow_sum(l);
    if (lane < 16) *reinterpret_cast<f32x4*>(lds + L_ML + (wave * 16 + lane) * 16) = f32x4{m * QSCALE, l, accz[0], 0.f};
    unsigned char* orow = lds + L_MRG + (wave * 16 + c) * OROW + g * 16;
#pragma unroll
    for (int h = 0; h < 2; ++h)
#pragma unroll
      for (int e = 0; e < 4; ++e) *reinterpret_cast<f32x4*>(orow + h * 256 + e * 64) = acc[h][e];
  }
  __syncthreads();
  STAMP(2);
  {
    const int h = tid >> 5, d4 = tid & 31;
    f32x4 ml[NW]; float zl[NW]; f32x4 a[NW], b[NW];
#pragma unroll
    for (int w = 0; w < NW; ++w) {
      ml[w] = *reinterpret_cast<const f32x4*>(lds + L_ML + (w * 16 + h) * 16);
      zl[w] = *reinterpret_cast<const float*>(lds + L_ML + (w * 16 + h + 8) * 16 + 8);
    }
#pragma unroll
    for (int w = 0; w < NW; ++w) {
      a[w] = *reinterpret_cast<const f32x4*>(lds + L_MRG + (w * 16 + h) * OROW + d4 * 16);
      b[w] = *reinterpret_cast<const f32x4*>(lds + L_MRG + (w * 16 + h + 8) * OROW + d4 * 16);
    }
    const float M = fmaxf(fmaxf(ml[0][0], ml[1][0]), fmaxf(ml[2][0], ml[3][0]));
    float L = 0.f; f32x4 o = {0.f, 0.f, 0.f, 0.f};
#pragma unroll
    for (int w = 0; w < NW; ++w) {
      const float wt = __builtin_amdgcn_exp2f(ml[w][0] - M);
      L = fmaf(wt, ml[w][1], L);
      o += ((a[w] + ml[w][2]) + (b[w] + zl[w])) * wt;
    }
    const int po = ((tid >> 3) * NSPLIT + split) * PKT + (tid & 7) * 16;
    __builtin_amdgcn_raw_buffer_store_b128(u32x4{__float_as_uint(o[0]), tagA, __float_as_uint(o[1]), tagB}, rp, po, 0, AUX_SC1);
    __builtin_amdgcn_raw_buffer_store_b128(u32x4{__float_as_uint(o[2]), tagA, __float_as_uint(o[3]), tagB}, rp, po + 128, 0, AUX_SC1);
    __builtin_amdgcn_raw_buffer_store_b128(u32x4{__float_as_uint(M), tagA, __float_as_uint(L), tagB}, rp, d4 == 0 ? WS_ML + (h * NSPLIT + split) * 16 : OOB, 0, AUX_SC1);
    SB();
  }
  STAMP(3);
  {
    const int ib = (split * NSPLIT + 8 * wave + g) * PKT + c * 16, im = WS_ML + ((split >> 2) * NSPLIT + 8 * wave + g) * 16;
    float d0[2], d1[2], pm[2], pl[2];
    bool pend[2] = {true, true};
#pragma unroll
    for (int j = 0; j < 2; ++j) { d0[j] = 0.f; d1[j] = 0.f; pm[j] = 0.f; pl[j] = 0.f; }
    for (int spins = 0;; ++spins) {
      asm volatile("" ::: "memory");
      u32x4 x[2], y[2];
#pragma unroll
      for (int j = 0; j < 2; ++j) {
        x[j] = __builtin_amdgcn_raw_buffer_load_b128(rp, pend[j] ? ib + j * 4 * PKT : OOB, 0, AUX_SC1);
        y[j] = __builtin_amdgcn_raw_buffer_load_b128(rp, pend[j] ? im + j * 64 : OOB, 0, AUX_SC1);
      }
#pragma unroll
      for (int j = 0; j < 2; ++j) {
        const bool ok = pend[j] & ((((x[j][1] ^ tagA) | (x[j][3] ^ tagB)) | ((y[j][1] ^ tagA) | (y[j][3] ^ tagB))) == 0u);
        d0[j] = ok ? __uint_as_float(x[j][0]) : d0[j]; d1[j] = ok ? __uint_as_float(x[j][2]) : d1[j];
        pm[j] = ok ? __uint_as_float(y[j][0]) : pm[j]; pl[j] = ok ? __uint_as_float(y[j][2]) : pl[j];
        pend[j] = pend[j] & !ok;
      }
      const int nmiss = __builtin_popcountll(__builtin_amdgcn_ballot_w64(pend[0])) + __builtin_popcountll(__builtin_amdgcn_ballot_w64(pend[1]));
      if (nmiss == 0 || spins > (1 << 20)) break;
      if (nmiss > 32) __builtin_amdgcn_s_sleep(SLEEP_FAR);
    }
    const float M = xrow_max(fmaxf(pm[0], pm[1]));
    const float w0 = __builtin_amdgcn_exp2f(pm[0] - M), w1 = __builtin_amdgcn_exp2f(pm[1] - M);
    const float L = xrow_sum(fmaf(w0, pl[0], w1 * pl[1])), o0 = xrow_sum(fmaf(w0, d0[0], w1 * d0[1])), o1 = xrow_sum(fmaf(w0, d1[0], w1 * d1[1]));
    if (lane < 16) *reinterpret_cast<f32x2*>(lw + (4 * (c & 7) + 2 * (c >> 3)) * 4) = f32x2{o0, o1};
    if (lane == 16) *reinterpret_cast<f32x2*>(lw + 128) = f32x2{M, L};
    __syncthreads();
    if (tid < 16) {
      f32x2 r[NW]; float s0[NW], s1[NW];
#pragma unroll
      for (int w = 0; w < NW; ++w) {
        r[w] = *reinterpret_cast<const f32x2*>(lds + w * L_WAVE + 128);
        s0[w] = *reinterpret_cast<const float*>(lds + w * L_WAVE + tid * 4);
        s1[w] = *reinterpret_cast<const float*>(lds + w * L_WAVE + tid * 4 + 64);
      }
      const float MM = fmaxf(fmaxf(r[0][0], r[1][0]), fmaxf(r[2][0], r[3][0]));
      float LL = 0.f, a0 = 0.f, a1 = 0.f;
#pragma unroll
      for (int w = 0; w < NW; ++w) { const float wt = __builtin_amdgcn_exp2f(r[w][0] - MM); LL = fmaf(wt, r[w][1], LL); a0 = fmaf(wt, s0[w], a0); a1 = fmaf(wt, s1[w], a1); }
      const float inv = __builtin_amdgcn_rcpf(LL);
      outw[2 * tid] = pk_bf16(a0 * inv, a1 * inv);
      if (split == 0 && tid == 0) __builtin_amdgcn_raw_buffer_store_b64(u32x2{tagA, tagB}, rp, 0, WS_EPOCH, AUX_SC1);
    }
  }
}

extern "C" void kernel_launch(void* const* d_in, const int* in_sizes, int n_in, void* d_out, int out_size,
                              void* d_ws, size_t ws_size, hipStream_t stream) {
  (void)hipFuncSetAttribute((const void*)kivi_decode, hipFuncAttributeMaxDynamicSharedMemorySize, LDS_BYTES);
  kivi_decode<<<NBH * NSPLIT, NW * 64, LDS_BYTES, stream>>>((const u32*)d_in[0], (const int*)d_in[1], (const int*)d_in[2],
      (const u32*)d_in[3], (const u32*)d_in[4], (const u32*)d_in[5], (const u32*)d_in[6], (unsigned char*)d_ws, (u32*)d_out);
}
```

```cpp
#include <hip/hip_runtime.h>
#include <hip/hip_bf16.h>
#include <stdint.h>
#define PROBE 1
#define AMP 20
#define ONLY0 100

typedef unsigned u32;
typedef u32 u32x4 __attribute__((ext_vector_type(4)));
typedef u32 u32x2 __attribute__((ext_vector_type(2)));
typedef float f32x4 __attribute__((ext_vector_type(4)));
typedef float f32x2 __attribute__((ext_vector_type(2)));
typedef __bf16 bf16x8 __attribute__((ext_vector_type(8)));
typedef __bf16 bf16x2 __attribute__((ext_vector_type(2)));
typedef short s16x4 __attribute__((ext_vector_type(4)));
typedef __amdgpu_buffer_rsrc_t rsrc_t;

constexpr int B_ = 2, HQ = 32, HKV = 4, SKV = 32768, DH = 128, GT = 128, NGK = SKV / GT;
constexpr int REP = HQ / HKV;
constexpr int NBH = B_ * HKV;
constexpr int NW = 4;
constexpr int NSPLIT = 32;
#define NE 17
#define SH 3
#define SO 1
constexpr float QSCALE = 0.088388347648318440f * 1.4426950408889634f;

constexpr int KROW = 272, L_WAVE = 16 * KROW;
constexpr int OROW = (DH + 4) * 4;
constexpr int L_MRG = NW * L_WAVE, L_ML = L_MRG + NW * 16 * OROW, L_END = L_ML + NW * 16 * 16;
constexpr int LDS_BYTES = 84 * 1024;
static_assert(L_END <= LDS_BYTES, "LDS map");

constexpr int PKT = 256, WS_ML = NSPLIT * NSPLIT * PKT, WS_EPOCH = WS_ML + REP * NSPLIT * 16, WS_GRP = 512 * 1024;
constexpr int OOB = 0x7fffffe0;
constexpr int AUX_NT = 2, AUX_SC1 = 16;
#define SLEEP_FAR 8

#define STAMP(i)

__device__ __forceinline__ float bf_lo(u32 x) { return __builtin_bit_cast(float, x << 16); }
__device__ __forceinline__ float bf_hi(u32 x) { return __builtin_bit_cast(float, x & 0xffff0000u); }
__device__ __forceinline__ u32 pk_bf16(float a, float b) {
  bf16x2 r; r[0] = (__bf16)a; r[1] = (__bf16)b; return __builtin_bit_cast(u32, r);
}
__device__ __forceinline__ float i2f(u32 x) { return (float)(int)x; }
__device__ __forceinline__ float xrow_max(float x) {
  auto s = __builtin_amdgcn_permlane16_swap(__float_as_uint(x), __float_as_uint(x), false, false);
  x = fmaxf(__uint_as_float(s[0]), __uint_as_float(s[1]));
  auto t = __builtin_amdgcn_permlane32_swap(__float_as_uint(x), __float_as_uint(x), false, false);
  return fmaxf(__uint_as_float(t[0]), __uint_as_float(t[1]));
}
__device__ __forceinline__ float xrow_sum(float x) {
  auto s = __builtin_amdgcn_permlane16_swap(__float_as_uint(x), __float_as_uint(x), false, false);
  x = __uint_as_float(s[0]) + __uint_as_float(s[1]);
  auto t = __builtin_amdgcn_permlane32_swap(__float_as_uint(x), __float_as_uint(x), false, false);
  return __uint_as_float(t[0]) + __uint_as_float(t[1]);
}
#define SB() __builtin_amdgcn_sched_barrier(0)

__global__ void __launch_bounds__(NW * 64, 2)
kivi_decode(const u32* __restrict__ q, const int* __restrict__ qk, const int* __restrict__ qv,
            const u32* __restrict__ ks, const u32* __restrict__ kz,
            const u32* __restrict__ vs, const u32* __restrict__ vz, unsigned char* __restrict__ ws, u32* __restrict__ out) {
  extern __shared__ __attribute__((aligned(16))) unsigned char lds[];
  unsigned long long tstart, tx = 0;
#define RT(t_) do { __builtin_amdgcn_sched_barrier(0); asm volatile("s_memrealtime %0\n\ts_waitcnt lgkmcnt(0)" : "=s"(t_) :: "memory"); __builtin_amdgcn_sched_barrier(0); } while (0)
  asm volatile("s_memrealtime %0" : "=s"(tstart) :: "memory");
  const int tid = threadIdx.x, lane = tid & 63;
  const int wave = __builtin_amdgcn_readfirstlane(tid >> 6);
  const int c = lane & 15, g = lane >> 4;
  const int side = blockIdx.x & 1, idx = blockIdx.x >> 3;
  const int bh = (blockIdx.x & 6) + (idx >> 4), rank = idx & 15, split = 2 * rank + side;
  const int nr = side ? 32 - NE : NE;
  const int t0 = (side ? NE * 64 : 0) + rank * 4 + wave;
  int rnd = SH * bh + SO * side;
  static_assert(SH * 7 + SO < 3 * (NE < 16 ? NE : 32 - NE), "start round");
  rnd -= rnd >= nr ? nr : 0; rnd -= rnd >= nr ? nr : 0;
  const char* kb = (const char*)qk + (size_t)bh * SKV * (DH * 4);
  const char* vb = (const char*)qv + (size_t)bh * SKV * (DH * 4);
  const rsrc_t rks = __builtin_amdgcn_make_buffer_rsrc((void*)(ks + bh * (SKV / 2)), 0, SKV * 2, 0x00020000);
  const rsrc_t rkz = __builtin_amdgcn_make_buffer_rsrc((void*)(kz + bh * (SKV / 2)), 0, SKV * 2, 0x00020000);
  const rsrc_t rvs = __builtin_amdgcn_make_buffer_rsrc((void*)(vs + bh * (SKV / 2)), 0, SKV * 2, 0x00020000);
  const rsrc_t rvz = __builtin_amdgcn_make_buffer_rsrc((void*)(vz + bh * (SKV / 2)), 0, SKV * 2, 0x00020000);
  const int voff = g * 2048 + c * 16;
  unsigned char* lw = lds + wave * L_WAVE;
  const rsrc_t rp = __builtin_amdgcn_make_buffer_rsrc((void*)(ws + (size_t)bh * WS_GRP), 0, WS_EPOCH + 8, 0x00020000);
  u32* outw = out + ((bh * REP + (split >> 2)) * DH + ((split >> 1) & 1) * 64 + (split & 1) * 2) / 2;
  u32x4 K[8], V[8];
  u32x2 ksr[2], kzr[2], vsr, vzr;
  bf16x8 qf[4];
  u32x2 epoch;
  u32 touch;
#define LOAD_PARAMS(tile_) do { \
    ksr[0] = __builtin_amdgcn_raw_buffer_load_b64(rks, c * 8, ((tile_) >> 3) * 256, 0); ksr[1] = __builtin_amdgcn_raw_buffer_load_b64(rks, c * 8 + 128, ((tile_) >> 3) * 256, 0); \
    kzr[0] = __builtin_amdgcn_raw_buffer_load_b64(rkz, c * 8, ((tile_) >> 3) * 256, 0); kzr[1] = __builtin_amdgcn_raw_buffer_load_b64(rkz, c * 8 + 128, ((tile_) >> 3) * 256, 0); \
    vsr = __builtin_amdgcn_raw_buffer_load_b64(rvs, g * 8, (tile_) * 32, 0); vzr = __builtin_amdgcn_raw_buffer_load_b64(rvz, g * 8, (tile_) * 32, 0); } while (0)
  {
    const rsrc_t rk = __builtin_amdgcn_make_buffer_rsrc((void*)kb, 0, SKV * DH * 4, 0x00020000);
    const rsrc_t rv = __builtin_amdgcn_make_buffer_rsrc((void*)vb, 0, SKV * DH * 4, 0x00020000);
    const int tile = t0 + rnd * 64;
    LOAD_PARAMS(tile);
    SB();
    RT(tx);
#pragma unroll
    for (int n = 0; n < 8; ++n) K[n] = __builtin_amdgcn_raw_buffer_load_b128(rk, voff + (n >> 1) * 512 + (n & 1) * 256, tile * 8192, AUX_NT);
    SB();
    const u32* qp = q + ((size_t)bh * REP + (c & 7)) * (DH / 2) + 4 * g;
#pragma unroll
    for (int s = 0; s < 4; ++s) qf[s] = __builtin_bit_cast(bf16x8, *reinterpret_cast<const u32x4*>(qp + 16 * s));
    SB();
#pragma unroll
    for (int n = 0; n < 8; ++n) V[n] = __builtin_amdgcn_raw_buffer_load_b128(rv, voff + (n >> 1) * 512 + (n & 1) * 256, tile * 8192, AUX_NT);
    SB();
    epoch = __builtin_amdgcn_raw_buffer_load_b64(rp, 0, WS_EPOCH, AUX_SC1);
    touch = __builtin_amdgcn_raw_buffer_load_b32(rp, lane < 16 ? (split * NSPLIT + 8 * wave) * PKT + lane * 128 : (tid == 16 ? WS_ML + split * 128 : OOB), 0, AUX_SC1);
    touch += outw[2 * c];
  }
  SB();

  unsigned char* kt_w = lw + g * (4 * KROW) + c * 8;
  const unsigned char* kt_r = lw + c * KROW + g * 16;
  const u32 lomask = c >= 8 ? 0xffff0000u : 0u;
  f32x4 acc[2][4], accz = {0.f, 0.f, 0.f, 0.f};
#pragma unroll
  for (int h = 0; h < 2; ++h)
#pragma unroll
    for (int e = 0; e < 4; ++e) acc[h][e] = f32x4{0.f, 0.f, 0.f, 0.f};
  float m = -INFINITY, l = 0.f;

#pragma unroll 1
  for (int it = 0; it < nr; ++it) {
    const bool more = it + 1 < nr;
    rnd = rnd + 1 == nr ? 0 : rnd + 1;
    const int tile = t0 + rnd * 64;
    const int soff = tile * 8192;
    const rsrc_t rk = __builtin_amdgcn_make_buffer_rsrc((void*)kb, 0, more ? SKV * DH * 4 : 0, 0x00020000);
    const rsrc_t rv = __builtin_amdgcn_make_buffer_rsrc((void*)vb, 0, more ? SKV * DH * 4 : 0, 0x00020000);
    f32x4 sc[2], zc[2];
#pragma unroll
    for (int h = 0; h < 2; ++h) {
      sc[h] = f32x4{bf_lo(ksr[h][0]), bf_hi(ksr[h][0]), bf_lo(ksr[h][1]), bf_hi(ksr[h][1])};
      zc[h] = -f32x4{bf_lo(kzr[h][0]), bf_hi(kzr[h][0]), bf_lo(kzr[h][1]), bf_hi(kzr[h][1])} * sc[h];
    }
    f32x4 vsc = {bf_lo(vsr[0]), bf_hi(vsr[0]), bf_lo(vsr[1]), bf_hi(vsr[1])};
    u32x2 vzn = vzr ^ 0x80008000u;
    asm volatile("" : "+v"(sc[0]), "+v"(sc[1]), "+v"(zc[0]), "+v"(zc[1]), "+v"(vsc), "+v"(vzn));
    SB();
    LOAD_PARAMS(tile);
    SB();
#pragma unroll
    for (int n = 0; n < 8; ++n) {
      const int h = n & 1;
      const u32x2 w = {pk_bf16(fmaf(i2f(K[n][0]), sc[h][0], zc[h][0]), fmaf(i2f(K[n][1]), sc[h][1], zc[h][1])),
                       pk_bf16(fmaf(i2f(K[n][2]), sc[h][2], zc[h][2]), fmaf(i2f(K[n][3]), sc[h][3], zc[h][3]))};
      *reinterpret_cast<u32x2*>(kt_w + (n >> 1) * KROW + h * 128) = w;
      SB();
      K[n] = __builtin_amdgcn_raw_buffer_load_b128(rk, voff + (n >> 1) * 512 + h * 256, soff, AUX_NT);
      SB();
    }
    bf16x8 kf[4];
#pragma unroll
    for (int s = 0; s < 4; ++s) kf[s] = __builtin_bit_cast(bf16x8, *reinterpret_cast<const u32x4*>(kt_r + 64 * s));
    f32x4 S0 = {0.f, 0.f, 0.f, 0.f}, S1 = {0.f, 0.f, 0.f, 0.f};
    S0 = __builtin_amdgcn_mfma_f32_16x16x32_bf16(kf[0], qf[0], S0, 0, 0, 0);
    S1 = __builtin_amdgcn_mfma_f32_16x16x32_bf16(kf[1], qf[1], S1, 0, 0, 0);
    S0 = __builtin_amdgcn_mfma_f32_16x16x32_bf16(kf[2], qf[2], S0, 0, 0, 0);
    S1 = __builtin_amdgcn_mfma_f32_16x16x32_bf16(kf[3], qf[3], S1, 0, 0, 0);
    const f32x4 S = S0 + S1;
    const float mn = fmaxf(m, xrow_max(fmaxf(fmaxf(S[0], S[1]), fmaxf(S[2], S[3]))));
    const float alpha = __builtin_amdgcn_exp2f((m - mn) * QSCALE);
    const float mc = mn * QSCALE;
    m = mn;
    float p[4];
#pragma unroll
    for (int r = 0; r < 4; ++r) p[r] = __builtin_amdgcn_exp2f(fmaf(S[r], QSCALE, -mc));
    l = fmaf(l, alpha, (p[0] + p[1]) + (p[2] + p[3]));
    u32 y[4];
#pragma unroll
    for (int r = 0; r < 4; ++r) {
      const float x = p[r] * vsc[r];
      y[r] = __float_as_uint(x - __uint_as_float(__float_as_uint(x) & lomask));
    }
    const u32x2 pfu = {__builtin_amdgcn_perm(y[1], y[0], 0x07060302u), __builtin_amdgcn_perm(y[3], y[2], 0x07060302u)};
    const s16x4 pf = __builtin_bit_cast(s16x4, pfu);
#pragma unroll
    for (int h = 0; h < 2; ++h)
#pragma unroll
      for (int e = 0; e < 4; ++e) acc[h][e] *= alpha;
    accz = __builtin_amdgcn_mfma_f32_16x16x16bf16_1k(__builtin_bit_cast(s16x4, vzn), pf, accz * alpha, 0, 0, 0);
    SB();
    u32x2 vf[2][4];
#pragma unroll
    for (int mp = 0; mp < 2; ++mp) {
#pragma unroll
      for (int h = 0; h < 2; ++h)
#pragma unroll
        for (int e = 0; e < 4; ++e) vf[h][e][mp] = pk_bf16(i2f(V[4 * mp + h][e]), i2f(V[4 * mp + h + 2][e]));
      SB();
#pragma unroll
      for (int n = 4 * mp; n < 4 * mp + 4; ++n) V[n] = __builtin_amdgcn_raw_buffer_load_b128(rv, voff + (n >> 1) * 512 + (n & 1) * 256, soff, AUX_NT);
      SB();
    }
#pragma unroll
    for (int h = 0; h < 2; ++h)
#pragma unroll
      for (int e = 0; e < 4; ++e)
        acc[h][e] = __builtin_amdgcn_mfma_f32_16x16x16bf16_1k(__builtin_bit_cast(s16x4, vf[h][e]), pf, acc[h][e], 0, 0, 0);
    SB();
  }
  STAMP(1);
  asm volatile("" :: "v"(touch));
  const u32 tagA = epoch[0] + 1u, tagB = epoch[1] + 1u;

  {
    l = xrow_sum(l);
    if (lane < 16) *reinterpret_cast<f32x4*>(lds + L_ML + (wave * 16 + lane) * 16) = f32x4{m * QSCALE, l, accz[0], 0.f};
    unsigned char* orow = lds + L_MRG + (wave * 16 + c) * OROW + g * 16;
#pragma unroll
    for (int h = 0; h < 2; ++h)
#pragma unroll
      for (int e = 0; e < 4; ++e) *reinterpret_cast<f32x4*>(orow + h * 256 + e * 64) = acc[h][e];
  }
  __syncthreads();
  STAMP(2);
  {
    const int h = tid >> 5, d4 = tid & 31;
    f32x4 ml[NW]; float zl[NW]; f32x4 a[NW], b[NW];
#pragma unroll
    for (int w = 0; w < NW; ++w) {
      ml[w] = *reinterpret_cast<const f32x4*>(lds + L_ML + (w * 16 + h) * 16);
      zl[w] = *reinterpret_cast<const float*>(lds + L_ML + (w * 16 + h + 8) * 16 + 8);
    }
#pragma unroll
    for (int w = 0; w < NW; ++w) {
      a[w] = *reinterpret_cast<const f32x4*>(lds + L_MRG + (w * 16 + h) * OROW + d4 * 16);
      b[w] = *reinterpret_cast<const f32x4*>(lds + L_MRG + (w * 16 + h + 8) * OROW + d4 * 16);
    }
    const float M = fmaxf(fmaxf(ml[0][0], ml[1][0]), fmaxf(ml[2][0], ml[3][0]));
    float L = 0.f; f32x4 o = {0.f, 0.f, 0.f, 0.f};
#pragma unroll
    for (int w = 0; w < NW; ++w) {
      const float wt = __builtin_amdgcn_exp2f(ml[w][0] - M);
      L = fmaf(wt, ml[w][1], L);
      o += ((a[w] + ml[w][2]) + (b[w] + zl[w])) * wt;
    }
    const int po = ((tid >> 3) * NSPLIT + split) * PKT + (tid & 7) * 16;
    __builtin_amdgcn_raw_buffer_store_b128(u32x4{__float_as_uint(o[0]), tagA, __float_as_uint(o[1]), tagB}, rp, po, 0, AUX_SC1);
    __builtin_amdgcn_raw_buffer_store_b128(u32x4{__float_as_uint(o[2]), tagA, __float_as_uint(o[3]), tagB}, rp, po + 128, 0, AUX_SC1);
    __builtin_amdgcn_raw_buffer_store_b128(u32x4{__float_as_uint(M), tagA, __float_as_uint(L), tagB}, rp, d4 == 0 ? WS_ML + (h * NSPLIT + split) * 16 : OOB, 0, AUX_SC1);
    SB();
  }
  STAMP(3);
  {
    const int ib = (split * NSPLIT + 8 * wave + g) * PKT + c * 16, im = WS_ML + ((split >> 2) * NSPLIT + 8 * wave + g) * 16;
    float d0[2], d1[2], pm[2], pl[2];
    bool pend[2] = {true, true};
#pragma unroll
    for (int j = 0; j < 2; ++j) { d0[j] = 0.f; d1[j] = 0.f; pm[j] = 0.f; pl[j] = 0.f; }
    for (int spins = 0;; ++spins) {
      asm volatile("" ::: "memory");
      u32x4 x[2], y[2];
#pragma unroll
      for (int j = 0; j < 2; ++j) {
        x[j] = __builtin_amdgcn_raw_buffer_load_b128(rp, pend[j] ? ib + j * 4 * PKT : OOB, 0, AUX_SC1);
        y[j] = __builtin_amdgcn_raw_buffer_load_b128(rp, pend[j] ? im + j * 64 : OOB, 0, AUX_SC1);
      }
#pragma unroll
      for (int j = 0; j < 2; ++j) {
        const bool ok = pend[j] & ((((x[j][1] ^ tagA) | (x[j][3] ^ tagB)) | ((y[j][1] ^ tagA) | (y[j][3] ^ tagB))) == 0u);
        d0[j] = ok ? __uint_as_float(x[j][0]) : d0[j]; d1[j] = ok ? __uint_as_float(x[j][2]) : d1[j];
        pm[j] = ok ? __uint_as_float(y[j][0]) : pm[j]; pl[j] = ok ? __uint_as_float(y[j][2]) : pl[j];
        pend[j] = pend[j] & !ok;
      }
      const int nmiss = __builtin_popcountll(__builtin_amdgcn_ballot_w64(pend[0])) + __builtin_popcountll(__builtin_amdgcn_ballot_w64(pend[1]));
      if (nmiss == 0 || spins > (1 << 20)) break;
      if (nmiss > 32) __builtin_amdgcn_s_sleep(SLEEP_FAR);
    }
    const float M = xrow_max(fmaxf(pm[0], pm[1]));
    const float w0 = __builtin_amdgcn_exp2f(pm[0] - M), w1 = __builtin_amdgcn_exp2f(pm[1] - M);
    const float L = xrow_sum(fmaf(w0, pl[0], w1 * pl[1])), o0 = xrow_sum(fmaf(w0, d0[0], w1 * d0[1])), o1 = xrow_sum(fmaf(w0, d1[0], w1 * d1[1]));
    if (lane < 16) *reinterpret_cast<f32x2*>(lw + (4 * (c & 7) + 2 * (c >> 3)) * 4) = f32x2{o0, o1};
    if (lane == 16) *reinterpret_cast<f32x2*>(lw + 128) = f32x2{M, L};
    __syncthreads();
    if (tid < 16) {
      f32x2 r[NW]; float s0[NW], s1[NW];
#pragma unroll
      for (int w = 0; w < NW; ++w) {
        r[w] = *reinterpret_cast<const f32x2*>(lds + w * L_WAVE + 128);
        s0[w] = *reinterpret_cast<const float*>(lds + w * L_WAVE + tid * 4);
        s1[w] = *reinterpret_cast<const float*>(lds + w * L_WAVE + tid * 4 + 64);
      }
      const float MM = fmaxf(fmaxf(r[0][0], r[1][0]), fmaxf(r[2][0], r[3][0]));
      float LL = 0.f, a0 = 0.f, a1 = 0.f;
#pragma unroll
      for (int w = 0; w < NW; ++w) { const float wt = __builtin_amdgcn_exp2f(r[w][0] - MM); LL = fmaf(wt, r[w][1], LL); a0 = fmaf(wt, s0[w], a0); a1 = fmaf(wt, s1[w], a1); }
      const float inv = __builtin_amdgcn_rcpf(LL);
      outw[2 * tid] = pk_bf16(a0 * inv, a1 * inv);
      if (split == 0 && tid == 0) __builtin_amdgcn_raw_buffer_store_b64(u32x2{tagA, tagB}, rp, 0, WS_EPOCH, AUX_SC1);
    }
  }
  { const unsigned long long lim = 10000ull + AMP * (tx - tstart);
    if (blockIdx.x == ONLY0 && wave == 0)
    while (__builtin_amdgcn_s_memrealtime() - tstart < lim) __builtin_amdgcn_s_sleep(1); }
}

extern "C" void kernel_launch(void* const* d_in, const int* in_sizes, int n_in, void* d_out, int out_size,
                              void* d_ws, size_t ws_size, hipStream_t stream) {
  (void)hipFuncSetAttribute((const void*)kivi_decode, hipFuncAttributeMaxDynamicSharedMemorySize, LDS_BYTES);
  kivi_decode<<<NBH * NSPLIT, NW * 64, LDS_BYTES, stream>>>((const u32*)d_in[0], (const int*)d_in[1], (const int*)d_in[2],
      (const u32*)d_in[3], (const u32*)d_in[4], (const u32*)d_in[5], (const u32*)d_in[6], (unsigned char*)d_ws, (u32*)d_out);
}
```

```cpp
#include <hip/hip_runtime.h>
#include <hip/hip_bf16.h>
#include <stdint.h>
#define KQ 1

typedef unsigned u32;
typedef u32 u32x4 __attribute__((ext_vector_type(4)));
typedef u32 u32x2 __attribute__((ext_vector_type(2)));
typedef float f32x4 __attribute__((ext_vector_type(4)));
typedef float f32x2 __attribute__((ext_vector_type(2)));
typedef __bf16 bf16x8 __attribute__((ext_vector_type(8)));
typedef __bf16 bf16x2 __attribute__((ext_vector_type(2)));
typedef _Float16 f16x2 __attribute__((ext_vector_type(2)));
typedef short s16x4 __attribute__((ext_vector_type(4)));
typedef __amdgpu_buffer_rsrc_t rsrc_t;

constexpr int B_ = 2, HQ = 32, HKV = 4, SKV = 32768, DH = 128, GT = 128, NGK = SKV / GT;
constexpr int REP = HQ / HKV;
constexpr int NBH = B_ * HKV;
constexpr int NW = 4;
constexpr int NSPLIT = 32;
constexpr int NS = 17, IP = 3, ID = 9;
#define SH 3
#define SO 1
constexpr float QSCALE = 0.088388347648318440f * 1.4426950408889634f;

constexpr int NMAX = NS;
constexpr int KROW = 272, L_S = 16 * KROW, SREC = 1024 + 64, L_WAVE = L_S + NMAX * SREC;
constexpr int OROW = (DH + 4) * 4;
static_assert(16 * OROW <= NMAX * SREC, "merge rows");
constexpr int L_ML = NW * L_WAVE, L_Q = L_ML + NW * 16 * 16, L_END = L_Q + REP * KROW;
constexpr int LDS_BYTES = 96 * 1024;
static_assert(L_END <= LDS_BYTES, "LDS map");

constexpr int PKT = 128, WS_EPOCH = NSPLIT * NSPLIT * PKT, WS_GRP = 256 * 1024;
constexpr int OOB = 0x7fffffe0;
constexpr int AUX_NT = 2, AUX_SC1 = 16;
#define WIN 12
#define SLEEP_FAR 2
#define NEAR 2
#define PEEK 1

#define STAMP(i)

__device__ __forceinline__ float bf_lo(u32 x) { return __builtin_bit_cast(float, x << 16); }
__device__ __forceinline__ float bf_hi(u32 x) { return __builtin_bit_cast(float, x & 0xffff0000u); }
__device__ __forceinline__ u32 pk_bf16(float a, float b) {
  bf16x2 r; r[0] = (__bf16)a; r[1] = (__bf16)b; return __builtin_bit_cast(u32, r);
}
__device__ __forceinline__ float i2f(u32 x) { return (float)(int)x; }
template <int CTRL> __device__ __forceinline__ u32 dppu(u32 x) { return (u32)__builtin_amdgcn_mov_dpp((int)x, CTRL, 0xf, 0xf, true); }
template <int CTRL> __device__ __forceinline__ float dpp(float x) {
  return __builtin_bit_cast(float, __builtin_amdgcn_mov_dpp(__builtin_bit_cast(int, x), CTRL, 0xf, 0xf, true));
}
__device__ __forceinline__ u32 pk_f16(float a, float b) {
  f16x2 r; r[0] = (_Float16)a; r[1] = (_Float16)b; return __builtin_bit_cast(u32, r);
}
__device__ __forceinline__ float xrow_max(float x) {
  auto s = __builtin_amdgcn_permlane16_swap(__float_as_uint(x), __float_as_uint(x), false, false);
  x = fmaxf(__uint_as_float(s[0]), __uint_as_float(s[1]));
  auto t = __builtin_amdgcn_permlane32_swap(__float_as_uint(x), __float_as_uint(x), false, false);
  return fmaxf(__uint_as_float(t[0]), __uint_as_float(t[1]));
}
__device__ __forceinline__ float xrow_sum(float x) {
  auto s = __builtin_amdgcn_permlane16_swap(__float_as_uint(x), __float_as_uint(x), false, false);
  x = __uint_as_float(s[0]) + __uint_as_float(s[1]);
  auto t = __builtin_amdgcn_permlane32_swap(__float_as_uint(x), __float_as_uint(x), false, false);
  return __uint_as_float(t[0]) + __uint_as_float(t[1]);
}
#define SB() __builtin_amdgcn_sched_barrier(0)

__global__ void __launch_bounds__(NW * 64, 2)
kivi_decode(const u32* __restrict__ q, const int* __restrict__ qk, const int* __restrict__ qv,
            const u32* __restrict__ ks, const u32* __restrict__ kz,
            const u32* __restrict__ vs, const u32* __restrict__ vz, unsigned char* ws, u32* __restrict__ out) {
  extern __shared__ __attribute__((aligned(16))) unsigned char lds[];
  STAMP(0);
  const int tid = threadIdx.x, lane = tid & 63;
  const int wave = __builtin_amdgcn_readfirstlane(tid >> 6);
  const int c = lane & 15, g = lane >> 4;
  const int side = blockIdx.x & 1, idx = blockIdx.x >> 3;
  const int bh = (blockIdx.x & 6) + (idx >> 4), rank = idx & 15, split = 2 * rank + side;
  const int nfull = 16 - side;
  const int tw = rank * 4 + wave;
  int rnd0 = SH * bh + SO * side;
  static_assert(SH * 7 + SO < 45, "start round");
  rnd0 -= rnd0 >= nfull ? nfull : 0; rnd0 -= rnd0 >= nfull ? nfull : 0;
  const char* kb = (const char*)qk + (size_t)bh * SKV * (DH * 4);
  const char* vb = (const char*)qv + (size_t)bh * SKV * (DH * 4);
  const rsrc_t rks = __builtin_amdgcn_make_buffer_rsrc((void*)(ks + bh * (SKV / 2)), 0, SKV * 2, 0x00020000);
  const rsrc_t rkz = __builtin_amdgcn_make_buffer_rsrc((void*)(kz + bh * (SKV / 2)), 0, SKV * 2, 0x00020000);
  const rsrc_t rvs = __builtin_amdgcn_make_buffer_rsrc((void*)(vs + bh * (SKV / 2)), 0, SKV * 2, 0x00020000);
  const rsrc_t rvz = __builtin_amdgcn_make_buffer_rsrc((void*)(vz + bh * (SKV / 2)), 0, SKV * 2, 0x00020000);
  const int voff = g * 2048 + c * 16;
  unsigned char* lw = lds + wave * L_WAVE;
  u32x4 R[16];
  u32x2 pr[2][6];
  bf16x8 qf[4];
  u32 ep0, ep1;
  struct Slot { int q0, nq; };
  auto slot_of = [&](int s) {
    const int i = s >= NS ? s - NS : s;
    int r = i - (i > IP ? 1 : 0) - (i > ID ? side : 0) + rnd0; r -= r >= nfull ? nfull : 0;
    Slot o = {((side ? 17 * 64 : 0) + r * 64 + tw) * 4, 4};
    if (i == IP) { o.q0 = (16 * 64 + tw) * 4 + (side ? KQ : 0); o.nq = side ? 4 - KQ : KQ; }
    if ((i == ID && side) || s >= 2 * NS) { o.q0 = 0; o.nq = 0; }
    return o; };
  auto rsrc_of = [&](int s, const Slot& o) { return __builtin_amdgcn_make_buffer_rsrc((void*)(s >= NS ? vb : kb), 0, (o.q0 + o.nq) * 2048, 0x00020000); };
  Slot s0 = slot_of(0), s1 = slot_of(1);
#define LOAD_BIG(dst_, rs_, so_, n_) dst_ = __builtin_amdgcn_raw_buffer_load_b128(rs_, voff + ((n_) >> 1) * 512 + ((n_) & 1) * 256, so_, AUX_NT)
#define LOAD_PARAMS(x_, q0_) do { \
    pr[x_][0] = __builtin_amdgcn_raw_buffer_load_b64(rks, c * 8, ((q0_) >> 5) * 256, 0); pr[x_][1] = __builtin_amdgcn_raw_buffer_load_b64(rks, c * 8 + 128, ((q0_) >> 5) * 256, 0); \
    pr[x_][2] = __builtin_amdgcn_raw_buffer_load_b64(rkz, c * 8, ((q0_) >> 5) * 256, 0); pr[x_][3] = __builtin_amdgcn_raw_buffer_load_b64(rkz, c * 8 + 128, ((q0_) >> 5) * 256, 0); \
    pr[x_][4] = __builtin_amdgcn_raw_buffer_load_b64(rvs, g * 8, (q0_) * 8, 0); pr[x_][5] = __builtin_amdgcn_raw_buffer_load_b64(rvz, g * 8, (q0_) * 8, 0); } while (0)
  {
    const rsrc_t rk = __builtin_amdgcn_make_buffer_rsrc((void*)kb, 0, SKV * DH * 4, 0x00020000);
    static_assert(IP > 1 && ID > 1, "the first two slots are whole tiles");
    const int ta = s0.q0, tb = s1.q0;
    const u32x2 qr = *reinterpret_cast<const u32x2*>(q + bh * (REP * DH / 2) + tid * 2);
    LOAD_PARAMS(0, ta);
    SB();
#pragma unroll
    for (int n = 0; n < 8; ++n) LOAD_BIG(R[n], rk, ta * 2048, n);
    SB();
    typedef const __attribute__((address_space(4))) u32 cu32;
    asm volatile("" : "+s"(ws));
    cu32* wsc = (cu32*)(ws + (size_t)bh * WS_GRP);
    ep0 = wsc[WS_EPOCH / 4]; ep1 = wsc[WS_EPOCH / 4 + 1];
    u32 tch[8];
#pragma unroll
    for (int i = 0; i < 8; ++i) tch[i] = wsc[(split * NSPLIT + 8 * wave + i) * (PKT / 4)];
    SB();
    LOAD_PARAMS(1, tb);
    SB();
#pragma unroll
    for (int n = 0; n < WIN - 8; ++n) LOAD_BIG(R[8 + n], rk, tb * 2048, n);
    SB();
    *reinterpret_cast<u32x2*>(lds + L_Q + (tid >> 5) * KROW + (tid & 31) * 8) = qr;
    asm volatile("" : "+s"(ep0), "+s"(ep1) : "s"(tch[0]), "s"(tch[1]), "s"(tch[2]), "s"(tch[3]), "s"(tch[4]), "s"(tch[5]), "s"(tch[6]), "s"(tch[7]));
    asm volatile("s_waitcnt lgkmcnt(0)" ::: "memory");
    __builtin_amdgcn_s_barrier();
    asm volatile("" ::: "memory");
#pragma unroll
    for (int s = 0; s < 4; ++s) qf[s] = __builtin_bit_cast(bf16x8, *reinterpret_cast<const u32x4*>(lds + L_Q + (c & 7) * KROW + s * 64 + g * 16));
  }
  SB();

  unsigned char* kt_w = lw + g * (4 * KROW) + c * 8;
  const unsigned char* kt_r = lw + c * KROW + g * 16;
  float m = -INFINITY;
#define REFILL(X, n_) do { if ((n_) + WIN < 16 + 0 * (X)) LOAD_BIG(R[8 * (1 - (X)) + (n_) + WIN - 8], rs1, so1, (n_) + WIN - 8); \
                           else LOAD_BIG(R[8 * (X) + (n_) + WIN - 16], rs2, so2, (n_) + WIN - 16); } while (0)
#define SLOT_ADDR(s_) const Slot s2 = slot_of((s_) + 2); const rsrc_t rs1 = rsrc_of((s_) + 1, s1), rs2 = rsrc_of((s_) + 2, s2); \
                      const int so1 = s1.q0 * 2048, so2 = s2.q0 * 2048; (void)rs1; (void)so1
#define K_TILE(X, s_) do { \
    SLOT_ADDR(s_); \
    f32x4 sc[2], zc[2]; \
    _Pragma("unroll") for (int h = 0; h < 2; ++h) { \
      sc[h] = f32x4{bf_lo(pr[X][h][0]), bf_hi(pr[X][h][0]), bf_lo(pr[X][h][1]), bf_hi(pr[X][h][1])}; \
      zc[h] = -f32x4{bf_lo(pr[X][2 + h][0]), bf_hi(pr[X][2 + h][0]), bf_lo(pr[X][2 + h][1]), bf_hi(pr[X][2 + h][1])} * sc[h]; } \
    *reinterpret_cast<u32x4*>(lw + L_S + (s_) * SREC + 1024 + g * 16) = u32x4{pr[X][4][0], pr[X][4][1], pr[X][5][0], pr[X][5][1]}; \
    asm volatile("" : "+v"(sc[0]), "+v"(sc[1]), "+v"(zc[0]), "+v"(zc[1])); \
    SB(); \
    LOAD_PARAMS(X, s2.q0); \
    SB(); \
    _Pragma("unroll") for (int n = 0; n < 8; ++n) { \
      const int h = n & 1; const u32x4 k4 = R[8 * (X) + n]; \
      const u32x2 w = {pk_bf16(fmaf(i2f(k4[0]), sc[h][0], zc[h][0]), fmaf(i2f(k4[1]), sc[h][1], zc[h][1])), \
                       pk_bf16(fmaf(i2f(k4[2]), sc[h][2], zc[h][2]), fmaf(i2f(k4[3]), sc[h][3], zc[h][3]))}; \
      *reinterpret_cast<u32x2*>(kt_w + (n >> 1) * KROW + h * 128) = w; \
      SB(); \
      REFILL(X, n); \
      SB(); } \
    bf16x8 kf[4]; \
    _Pragma("unroll") for (int i = 0; i < 4; ++i) kf[i] = __builtin_bit_cast(bf16x8, *reinterpret_cast<const u32x4*>(kt_r + 64 * i)); \
    f32x4 S0 = {0.f, 0.f, 0.f, 0.f}, S1 = {0.f, 0.f, 0.f, 0.f}; \
    S0 = __builtin_amdgcn_mfma_f32_16x16x32_bf16(kf[0], qf[0], S0, 0, 0, 0); \
    S1 = __builtin_amdgcn_mfma_f32_16x16x32_bf16(kf[1], qf[1], S1, 0, 0, 0); \
    S0 = __builtin_amdgcn_mfma_f32_16x16x32_bf16(kf[2], qf[2], S0, 0, 0, 0); \
    S1 = __builtin_amdgcn_mfma_f32_16x16x32_bf16(kf[3], qf[3], S1, 0, 0, 0); \
    const f32x4 S = g < s0.nq ? S0 + S1 : f32x4{-INFINITY, -INFINITY, -INFINITY, -INFINITY}; \
    *reinterpret_cast<f32x4*>(lw + L_S + (s_) * SREC + lane * 16) = S; \
    m = fmaxf(fmaxf(m, fmaxf(S[0], S[1])), fmaxf(S[2], S[3])); \
    s0 = s1; s1 = s2; \
    SB(); } while (0)
#pragma unroll 1
  for (int s = 0; s + 1 < NS; s += 2) {
    K_TILE(0, s);
    K_TILE(1, s + 1);
  }
  K_TILE(0, NS - 1);
  asm volatile("" :: "v"(pr[1][0]), "v"(pr[1][1]), "v"(pr[1][2]), "v"(pr[1][3]), "v"(pr[1][4]), "v"(pr[1][5]));
  m = xrow_max(m);
  const float mc = m * QSCALE;
  const u32 lomask = c >= 8 ? 0xffff0000u : 0u;
  f32x4 acc[2][4], accz = {0.f, 0.f, 0.f, 0.f};
#pragma unroll
  for (int h = 0; h < 2; ++h)
#pragma unroll
    for (int e = 0; e < 4; ++e) acc[h][e] = f32x4{0.f, 0.f, 0.f, 0.f};
  float l = 0.f;
#define V_TILE(X, s_) do { \
    SLOT_ADDR(s_); \
    const f32x4 S = *reinterpret_cast<const f32x4*>(lw + L_S + ((s_) - NS) * SREC + lane * 16); \
    const u32x4 vp = *reinterpret_cast<const u32x4*>(lw + L_S + ((s_) - NS) * SREC + 1024 + g * 16); \
    const f32x4 vsc = {bf_lo(vp[0]), bf_hi(vp[0]), bf_lo(vp[1]), bf_hi(vp[1])}; \
    const u32x2 vzn = {vp[2] ^ 0x80008000u, vp[3] ^ 0x80008000u}; \
    float p[4]; u32 y[4]; \
    _Pragma("unroll") for (int r = 0; r < 4; ++r) p[r] = __builtin_amdgcn_exp2f(fmaf(S[r], QSCALE, -mc)); \
    l += (p[0] + p[1]) + (p[2] + p[3]); \
    _Pragma("unroll") for (int r = 0; r < 4; ++r) {     \
      const float x = p[r] * vsc[r]; \
      y[r] = __float_as_uint(x - __uint_as_float(__float_as_uint(x) & lomask)); } \
    u32x2 pfu = {__builtin_amdgcn_perm(y[1], y[0], 0x07060302u), __builtin_amdgcn_perm(y[3], y[2], 0x07060302u)}; \
    accz = __builtin_amdgcn_mfma_f32_16x16x16bf16_1k(__builtin_bit_cast(s16x4, vzn), __builtin_bit_cast(s16x4, pfu), accz, 0, 0, 0); \
    asm volatile("" : "+v"(pfu), "+v"(l), "+v"(accz)); \
    const s16x4 pf = __builtin_bit_cast(s16x4, pfu); \
    SB(); \
    u32x2 vf[2][4]; \
    _Pragma("unroll") for (int mp = 0; mp < 2; ++mp) { \
      _Pragma("unroll") for (int h = 0; h < 2; ++h) \
        _Pragma("unroll") for (int e = 0; e < 4; ++e) vf[h][e][mp] = pk_bf16(i2f(R[8 * (X) + 4 * mp + h][e]), i2f(R[8 * (X) + 4 * mp + h + 2][e])); \
      SB(); \
      _Pragma("unroll") for (int n = 4 * mp; n < 4 * mp + 4; ++n) REFILL(X, n); \
      SB(); } \
    _Pragma("unroll") for (int h = 0; h < 2; ++h) \
      _Pragma("unroll") for (int e = 0; e < 4; ++e) \
        acc[h][e] = __builtin_amdgcn_mfma_f32_16x16x16bf16_1k(__builtin_bit_cast(s16x4, vf[h][e]), pf, acc[h][e], 0, 0, 0); \
    s1 = s2; \
    SB(); } while (0)
#pragma unroll 1
  for (int s = NS; s + 1 < 2 * NS; s += 2) {
    V_TILE(1, s);
    V_TILE(0, s + 1);
  }
  {
    u32x4 keep[8];
#pragma unroll
    for (int n = 0; n < 8; ++n) keep[n] = R[n];
    V_TILE(1, 2 * NS - 1);
    asm volatile("" :: "v"(keep[0]), "v"(keep[1]), "v"(keep[2]), "v"(keep[3]), "v"(keep[4]), "v"(keep[5]), "v"(keep[6]), "v"(keep[7]));
  }
  STAMP(1);
  const rsrc_t rp = __builtin_amdgcn_make_buffer_rsrc((void*)(ws + (size_t)bh * WS_GRP), 0, WS_EPOCH + 8, 0x00020000);
  u32* outw = out + ((bh * REP + (split >> 2)) * DH + ((split >> 1) & 1) * 64 + (split & 1) * 2) / 2;
  const u32 tagA = (ep0 + 1u) << 8, tagB = (ep1 + 1u) << 8;
  const int ib = (split * NSPLIT + 8 * wave) * PKT + lane * 16;
  u32x4 x = __builtin_amdgcn_raw_buffer_load_b128(rp, ib, 0, AUX_SC1);
  SB();

  {
    l = xrow_sum(l);
    if (lane < 16) *reinterpret_cast<f32x4*>(lds + L_ML + (wave * 16 + lane) * 16) = f32x4{mc, l, accz[0], 0.f};
    unsigned char* orow = lw + L_S + c * OROW + g * 16;
#pragma unroll
    for (int h = 0; h < 2; ++h)
#pragma unroll
      for (int e = 0; e < 4; ++e) *reinterpret_cast<f32x4*>(orow + h * 256 + e * 64) = acc[h][e];
  }
  __syncthreads();
  STAMP(2);
  {
    const int h = tid >> 5, d4 = tid & 31;
    f32x4 ml[NW]; float zl[NW]; f32x4 a[NW], b[NW];
#pragma unroll
    for (int w = 0; w < NW; ++w) {
      ml[w] = *reinterpret_cast<const f32x4*>(lds + L_ML + (w * 16 + h) * 16);
      zl[w] = *reinterpret_cast<const float*>(lds + L_ML + (w * 16 + h + 8) * 16 + 8);
    }
#pragma unroll
    for (int w = 0; w < NW; ++w) {
      a[w] = *reinterpret_cast<const f32x4*>(lds + w * L_WAVE + L_S + h * OROW + d4 * 16);
      b[w] = *reinterpret_cast<const f32x4*>(lds + w * L_WAVE + L_S + (h + 8) * OROW + d4 * 16);
    }
    SB();
    const float M = fmaxf(fmaxf(ml[0][0], ml[1][0]), fmaxf(ml[2][0], ml[3][0]));
    float L = 0.f; f32x4 o = {0.f, 0.f, 0.f, 0.f};
#pragma unroll
    for (int w = 0; w < NW; ++w) {
      const float wt = __builtin_amdgcn_exp2f(ml[w][0] - M);
      L = fmaf(wt, ml[w][1], L);
      o += ((a[w] + ml[w][2]) + (b[w] + zl[w])) * wt;
    }
    const float mx = fmaxf(fmaxf(fabsf(o[0]), fabsf(o[1])), fmaxf(fabsf(o[2]), fabsf(o[3])));
    const int ex = max(__builtin_amdgcn_frexp_expf(mx), -110) - 15;
    const u32 mlb = (__float_as_uint((tid & 4) ? L : M) >> (8 * (tid & 3))) & 0xffu;
    const u32x4 gr = {pk_f16(__builtin_amdgcn_ldexpf(o[0], -ex), __builtin_amdgcn_ldexpf(o[1], -ex)), tagA | mlb,
                      pk_f16(__builtin_amdgcn_ldexpf(o[2], -ex), __builtin_amdgcn_ldexpf(o[3], -ex)), tagB | (u32)(ex + 128)};
    __builtin_amdgcn_raw_buffer_store_b128(gr, rp, ((tid >> 3) * NSPLIT + split) * PKT + (tid & 7) * 16, 0, AUX_SC1);
    SB();
  }
  STAMP(3);
  {
    for (int spins = 0;; ++spins) {
      const bool need = (((x[1] ^ tagA) | (x[3] ^ tagB)) & 0xffffff00u) != 0u;
      const int nmiss = __builtin_popcountll(__builtin_amdgcn_ballot_w64(need));
      if (nmiss == 0 || spins > (1 << 20)) break;
      if (nmiss > 8 * NEAR && spins > 0) __builtin_amdgcn_s_sleep(SLEEP_FAR);
      asm volatile("" ::: "memory");
      const u32x4 t = __builtin_amdgcn_raw_buffer_load_b128(rp, need ? ib : OOB, 0, AUX_SC1);
      x = need ? t : x;
    }
    STAMP(5);
    u32 mb = (x[1] & 0xffu) << (8 * (lane & 3));
    mb |= dppu<0xB1>(mb); mb |= dppu<0x4E>(mb);
    const u32 mo = dppu<0x141>(mb);
    const float pm = __uint_as_float((lane & 4) ? mo : mb), pl = __uint_as_float((lane & 4) ? mb : mo);
    const u32 x0 = x[0], x1 = x[2], xe = x[3];
    const float M = xrow_max(fmaxf(pm, dpp<0x128>(pm)));
    const float w = __builtin_amdgcn_exp2f(pm - M), wx = __builtin_amdgcn_ldexpf(w, (int)(xe & 0xffu) - 128);
    const f16x2 h0 = __builtin_bit_cast(f16x2, x0), h1 = __builtin_bit_cast(f16x2, x1);
    float v[5] = {(float)h0[0] * wx, (float)h0[1] * wx, (float)h1[0] * wx, (float)h1[1] * wx, w * pl};
#pragma unroll
    for (int k = 0; k < 5; ++k) v[k] = xrow_sum(v[k] + dpp<0x128>(v[k]));
    if (lane < 8) *reinterpret_cast<f32x4*>(lw + lane * 16) = f32x4{v[0], v[1], v[2], v[3]};
    if (lane == 8) *reinterpret_cast<f32x2*>(lw + 128) = f32x2{M, v[4]};
    __syncthreads();
    if (tid < 16) {
      f32x2 r[NW]; float s0[NW], s1[NW];
#pragma unroll
      for (int w = 0; w < NW; ++w) {
        r[w] = *reinterpret_cast<const f32x2*>(lds + w * L_WAVE + 128);
        s0[w] = *reinterpret_cast<const float*>(lds + w * L_WAVE + tid * 4);
        s1[w] = *reinterpret_cast<const float*>(lds + w * L_WAVE + tid * 4 + 64);
      }
      const float MM = fmaxf(fmaxf(r[0][0], r[1][0]), fmaxf(r[2][0], r[3][0]));
      float LL = 0.f, a0 = 0.f, a1 = 0.f;
#pragma unroll
      for (int w = 0; w < NW; ++w) { const float wt = __builtin_amdgcn_exp2f(r[w][0] - MM); LL = fmaf(wt, r[w][1], LL); a0 = fmaf(wt, s0[w], a0); a1 = fmaf(wt, s1[w], a1); }
      const float inv = __builtin_amdgcn_rcpf(LL);
      outw[2 * tid] = pk_bf16(a0 * inv, a1 * inv);
      if (split == 0 && tid == 0) __builtin_amdgcn_raw_buffer_store_b64(u32x2{ep0 + 1u, ep1 + 1u}, rp, 0, WS_EPOCH, AUX_SC1);
    }
  }
}

extern "C" void kernel_launch(void* const* d_in, const int* in_sizes, int n_in, void* d_out, int out_size,
                              void* d_ws, size_t ws_size, hipStream_t stream) {
  (void)hipFuncSetAttribute((const void*)kivi_decode, hipFuncAttributeMaxDynamicSharedMemorySize, LDS_BYTES);
  kivi_decode<<<NBH * NSPLIT, NW * 64, LDS_BYTES, stream>>>((const u32*)d_in[0], (const int*)d_in[1], (const int*)d_in[2],
      (const u32*)d_in[3], (const u32*)d_in[4], (const u32*)d_in[5], (const u32*)d_in[6], (unsigned char*)d_ws, (u32*)d_out);
}
```

```cpp
#include <hip/hip_runtime.h>
#include <hip/hip_bf16.h>
#include <stdint.h>
#define TT 109

typedef unsigned u32;
typedef u32 u32x4 __attribute__((ext_vector_type(4)));
typedef u32 u32x2 __attribute__((ext_vector_type(2)));
typedef float f32x4 __attribute__((ext_vector_type(4)));
typedef float f32x2 __attribute__((ext_vector_type(2)));
typedef __bf16 bf16x8 __attribute__((ext_vector_type(8)));
typedef __bf16 bf16x2 __attribute__((ext_vector_type(2)));
typedef _Float16 f16x2 __attribute__((ext_vector_type(2)));
typedef short s16x4 __attribute__((ext_vector_type(4)));
typedef __amdgpu_buffer_rsrc_t rsrc_t;

constexpr int B_ = 2, HQ = 32, HKV = 4, SKV = 32768, DH = 128, GT = 128, NGK = SKV / GT;
constexpr int REP = HQ / HKV;
constexpr int NBH = B_ * HKV;
constexpr int NW = 4;
constexpr int NSPLIT = 32;
constexpr int NS = 16;
#define SH 3
#define SO 1
constexpr float QSCALE = 0.088388347648318440f * 1.4426950408889634f;

constexpr int KROW = 272, L_S = 16 * KROW, SREC = 1024, L_WAVE = L_S + NS * SREC;
constexpr int OROW = (DH + 4) * 4;
static_assert(16 * OROW <= NS * SREC, "merge rows");
constexpr int L_KS = NW * L_WAVE, L_KZ = L_KS + NS * 512, L_VS = L_KZ + NS * 512, L_VZ = L_VS + NS * 256;
constexpr int L_ML = L_VZ + NS * 128, L_Q = L_ML + NW * 16 * 16, L_END = L_Q + REP * KROW;
constexpr int LDS_BYTES = 116 * 1024;
static_assert(L_END <= LDS_BYTES, "LDS map");

constexpr int PKT = 128, WS_EPOCH = NSPLIT * NSPLIT * PKT, WS_GRP = 256 * 1024;
constexpr int OOB = 0x7fffffe0;
constexpr int AUX_NT = 2, AUX_SC1 = 16;
#define WIN 12
#define W0 5
#define W1 6
#define W2 7
#define W3 9
#define DL 40
#define T0 170
static_assert(WIN == 12, "the issue points below assume a look-ahead of 12 loads");
#define VMWAIT(n_) __builtin_amdgcn_s_waitcnt(0x0f70 | ((n_) & 15) | (((n_) >> 4) << 14))
#define CT 0
#define AUX_LD 16
#define AUX_ST 16
#define FIRST 4
#define SLEEP_FAR 2
#define NEAR 2
#define PEEK 1

#define STAMP(i)

__device__ __forceinline__ float bf_lo(u32 x) { return __builtin_bit_cast(float, x << 16); }
__device__ __forceinline__ float bf_hi(u32 x) { return __builtin_bit_cast(float, x & 0xffff0000u); }
__device__ __forceinline__ u32 pk_bf16(float a, float b) {
  bf16x2 r; r[0] = (__bf16)a; r[1] = (__bf16)b; return __builtin_bit_cast(u32, r);
}
__device__ __forceinline__ float i2f(u32 x) { return (float)(int)x; }
template <int CTRL> __device__ __forceinline__ u32 dppu(u32 x) { return (u32)__builtin_amdgcn_mov_dpp((int)x, CTRL, 0xf, 0xf, true); }
template <int CTRL> __device__ __forceinline__ float dpp(float x) {
  return __builtin_bit_cast(float, __builtin_amdgcn_mov_dpp(__builtin_bit_cast(int, x), CTRL, 0xf, 0xf, true));
}
__device__ __forceinline__ u32 pk_f16(float a, float b) {
  f16x2 r; r[0] = (_Float16)a; r[1] = (_Float16)b; return __builtin_bit_cast(u32, r);
}
__device__ __forceinline__ float xrow_max(float x) {
  auto s = __builtin_amdgcn_permlane16_swap(__float_as_uint(x), __float_as_uint(x), false, false);
  x = fmaxf(__uint_as_float(s[0]), __uint_as_float(s[1]));
  auto t = __builtin_amdgcn_permlane32_swap(__float_as_uint(x), __float_as_uint(x), false, false);
  return fmaxf(__uint_as_float(t[0]), __uint_as_float(t[1]));
}
__device__ __forceinline__ float xrow_sum(float x) {
  auto s = __builtin_amdgcn_permlane16_swap(__float_as_uint(x), __float_as_uint(x), false, false);
  x = __uint_as_float(s[0]) + __uint_as_float(s[1]);
  auto t = __builtin_amdgcn_permlane32_swap(__float_as_uint(x), __float_as_uint(x), false, false);
  return __uint_as_float(t[0]) + __uint_as_float(t[1]);
}
#define SB() __builtin_amdgcn_sched_barrier(0)

__global__ void __launch_bounds__(NW * 64, 2)
kivi_decode(const u32* q, const int* qk, const int* qv,
            const u32* ks, const u32* kz,
            const u32* vs, const u32* vz, unsigned char* ws, u32* out) {
  extern __shared__ __attribute__((aligned(16))) unsigned char lds[];
  STAMP(0);
  const u32 ctouch = 0;
  const int tid = threadIdx.x, lane = tid & 63;
  const int wave = __builtin_amdgcn_readfirstlane(tid >> 6);
  const int c = lane & 15, g = lane >> 4;
  const int side = blockIdx.x & 1, idx = blockIdx.x >> 3;
  const int bh = (blockIdx.x & 6) + (idx >> 4), rank = idx & 15, split = 2 * rank + side;
  const int rnd0 = SH * bh + SO * side;
  const int voff = g * 2048 + c * 16;
  unsigned char* lw = lds + wave * L_WAVE;
  u32x4 R[16];
  bf16x8 qf[4];
  u32 ep0, ep1, tstart;
  auto round_of = [&](int i) { return side * 16 + ((i + rnd0) & 15); };
  auto soff_of = [&](int s) { return (round_of(s) * 64 + rank * 4 + wave) * 8192; };
  const int ko = (round_of(tid >> 4) * 8 + (rank >> 1)) * 256 + (tid & 15) * 16;
  const int vo = (round_of((tid & 127) >> 3) * 16 + rank) * 128 + (tid & 7) * 16;
  const int so0 = soff_of(0);
  asm volatile("" : "+s"(q), "+s"(qk), "+s"(qv), "+s"(ks), "+s"(kz), "+s"(vs), "+s"(vz), "+s"(ws), "+s"(out) : "v"(ko), "v"(vo), "v"(voff), "s"(so0), "s"(bh));
  const char* kb = (const char*)qk + (size_t)bh * SKV * (DH * 4);
  const char* vb = (const char*)qv + (size_t)bh * SKV * (DH * 4);
  auto rsrc_of = [&](int s) { return __builtin_amdgcn_make_buffer_rsrc((void*)(s >= NS ? vb : kb), 0, SKV * DH * 4, 0x00020000); };
#define LOAD_BIG(dst_, rs_, so_, n_) dst_ = __builtin_amdgcn_raw_buffer_load_b128(rs_, voff + ((n_) >> 1) * 512 + ((n_) & 1) * 256, so_, AUX_NT)
  {
    const rsrc_t rk = __builtin_amdgcn_make_buffer_rsrc((void*)kb, 0, SKV * DH * 4, 0x00020000);
    SB();
#pragma unroll
    for (int n = 0; n < FIRST; ++n) LOAD_BIG(R[n], rk, so0, n);
    SB();
    unsigned long long ts = __builtin_amdgcn_s_memrealtime();
    const u32x4 ksr = __builtin_amdgcn_raw_buffer_load_b128(__builtin_amdgcn_make_buffer_rsrc((void*)(ks + bh * (SKV / 2)), 0, SKV * 2, 0x00020000), ko, 0, 0);
    const u32x4 kzr = __builtin_amdgcn_raw_buffer_load_b128(__builtin_amdgcn_make_buffer_rsrc((void*)(kz + bh * (SKV / 2)), 0, SKV * 2, 0x00020000), ko, 0, 0);
    u32x4 vr = __builtin_amdgcn_raw_buffer_load_b128(__builtin_amdgcn_make_buffer_rsrc((void*)((wave < 2 ? vs : vz) + bh * (SKV / 2)), 0, SKV * 2, 0x00020000), vo, 0, 0);
    const u32x2 qr = __builtin_amdgcn_raw_buffer_load_b64(__builtin_amdgcn_make_buffer_rsrc((void*)q, 0, B_ * HQ * DH * 2, 0x00020000), tid * 8, bh * (REP * DH * 2), 0);
    SB();
#pragma unroll
    for (int n = FIRST; n < 8; ++n) LOAD_BIG(R[n], rk, so0, n);
    SB();
    typedef const __attribute__((address_space(4))) u32 cu32;
    cu32* wsc = (cu32*)(ws + (size_t)bh * WS_GRP);
    ep0 = wsc[WS_EPOCH / 4]; ep1 = wsc[WS_EPOCH / 4 + 1];
    u32 tch[8];
#pragma unroll
    for (int i = 0; i < 8; ++i) tch[i] = wsc[(split * NSPLIT + 8 * wave + i) * (PKT / 4)];
    SB();
#pragma unroll
    for (int n = 0; n < WIN - 8; ++n) LOAD_BIG(R[8 + n], rk, soff_of(1), n);
    SB();
#define UNPK(x_, i_) f32x4{bf_lo(x_[2 * (i_)]), bf_hi(x_[2 * (i_)]), bf_lo(x_[2 * (i_) + 1]), bf_hi(x_[2 * (i_) + 1])}
#pragma unroll
    for (int i = 0; i < 2; ++i) {
      *reinterpret_cast<f32x4*>(lds + L_KS + tid * 32 + i * 16) = UNPK(ksr, i);
      *reinterpret_cast<f32x4*>(lds + L_KZ + tid * 32 + i * 16) = -UNPK(kzr, i) * UNPK(ksr, i);
    }
    asm volatile("" : "+v"(vr));
    if (wave < 2) {
#pragma unroll
      for (int i = 0; i < 2; ++i) *reinterpret_cast<f32x4*>(lds + L_VS + tid * 32 + i * 16) = UNPK(vr, i);
    } else *reinterpret_cast<u32x4*>(lds + L_VZ + (tid & 127) * 16) = vr ^ 0x80008000u;
    *reinterpret_cast<u32x2*>(lds + L_Q + (tid >> 5) * KROW + (tid & 31) * 8) = qr;
    asm volatile("" : "+s"(ep0), "+s"(ep1), "+s"(ts) : "s"(tch[0]), "s"(tch[1]), "s"(tch[2]), "s"(tch[3]), "s"(tch[4]), "s"(tch[5]), "s"(tch[6]), "s"(tch[7]), "v"(ctouch));
    tstart = (u32)ts;
    asm volatile("s_waitcnt lgkmcnt(0)" ::: "memory");
    __builtin_amdgcn_s_barrier();
    asm volatile("" ::: "memory");
#pragma unroll
    for (int i = 0; i < 4; ++i) qf[i] = __builtin_bit_cast(bf16x8, *reinterpret_cast<const u32x4*>(lds + L_Q + (c & 7) * KROW + i * 64 + g * 16));
  }
  SB();

  unsigned char* kt_w = lw + g * (4 * KROW) + c * 8;
  const unsigned char* kt_r = lw + c * KROW + g * 16;
  float m = -INFINITY;
  int late = 0;
#define REFILL(X, n_) do { if ((n_) + WIN < 16 + 0 * (X)) LOAD_BIG(R[8 * (1 - (X)) + (n_) + WIN - 8], rs1, so1, (n_) + WIN - 8); \
                           else LOAD_BIG(R[8 * (X) + (n_) + WIN - 16], rs2, so2, (n_) + WIN - 16); } while (0)
#define SLOT_ADDR(s_) const rsrc_t rs1 = rsrc_of((s_) + 1), rs2 = rsrc_of((s_) + 2); const int so1 = soff_of((s_) + 1), so2 = soff_of((s_) + 2); \
                      (void)rs1; (void)so1; (void)rs2; (void)so2
#define ISSUE(X, k_, on_) do { SB(); if (on_) { VMWAIT(W3 - 1); \
      asm volatile("s_cmp_gt_i32 %0, %4\n\ts_cbranch_scc1 1f\n\ts_waitcnt vmcnt(%1)\n\t" \
                   "s_cmp_gt_i32 %0, %5\n\ts_cbranch_scc1 1f\n\ts_waitcnt vmcnt(%2)\n\t" \
                   "s_cmp_gt_i32 %0, 0\n\ts_cbranch_scc1 1f\n\ts_waitcnt vmcnt(%3)\n1:" \
                   :: "s"(late), "i"(W2 - 1), "i"(W1 - 1), "i"(W0 - 1), "i"(2 * DL), "i"(DL) : "scc"); \
      REFILL(X, k_); } SB(); } while (0)
#define CLOCK_READ() unsigned long long now = __builtin_amdgcn_s_memrealtime()
#define CLOCK_USE(s_) do { asm volatile("" : "+s"(now)); late = (int)((u32)now - tstart) - (T0 + TT * (s_)); } while (0)
#define K_CVT(X, n_) do { const int h = (n_) & 1; const u32x4 k4 = R[8 * (X) + (n_)]; \
      const u32x2 w = {pk_bf16(fmaf(i2f(k4[0]), sc[h][0], zc[h][0]), fmaf(i2f(k4[1]), sc[h][1], zc[h][1])), \
                       pk_bf16(fmaf(i2f(k4[2]), sc[h][2], zc[h][2]), fmaf(i2f(k4[3]), sc[h][3], zc[h][3]))}; \
      *reinterpret_cast<u32x2*>(kt_w + ((n_) >> 1) * KROW + h * 128) = w; } while (0)
#define K_TILE(X, s_) do { \
    SLOT_ADDR(s_); \
    CLOCK_READ(); \
    f32x4 sc[2], zc[2]; \
    _Pragma("unroll") for (int h = 0; h < 2; ++h) { \
      sc[h] = *reinterpret_cast<const f32x4*>(lds + L_KS + (s_) * 512 + h * 256 + c * 16); \
      zc[h] = *reinterpret_cast<const f32x4*>(lds + L_KZ + (s_) * 512 + h * 256 + c * 16); } \
    ISSUE(X, 0, true); \
    K_CVT(X, 0); K_CVT(X, 1); CLOCK_USE(s_); ISSUE(X, 1, true); \
    K_CVT(X, 2); K_CVT(X, 3); ISSUE(X, 2, true); \
    K_CVT(X, 4); K_CVT(X, 5); ISSUE(X, 3, true); \
    K_CVT(X, 6); K_CVT(X, 7); ISSUE(X, 4, true); \
    bf16x8 kf[4]; \
    _Pragma("unroll") for (int i = 0; i < 4; ++i) kf[i] = __builtin_bit_cast(bf16x8, *reinterpret_cast<const u32x4*>(kt_r + 64 * i)); \
    ISSUE(X, 5, true); \
    f32x4 S0 = {0.f, 0.f, 0.f, 0.f}, S1 = {0.f, 0.f, 0.f, 0.f}; \
    S0 = __builtin_amdgcn_mfma_f32_16x16x32_bf16(kf[0], qf[0], S0, 0, 0, 0); \
    S1 = __builtin_amdgcn_mfma_f32_16x16x32_bf16(kf[1], qf[1], S1, 0, 0, 0); \
    S0 = __builtin_amdgcn_mfma_f32_16x16x32_bf16(kf[2], qf[2], S0, 0, 0, 0); \
    S1 = __builtin_amdgcn_mfma_f32_16x16x32_bf16(kf[3], qf[3], S1, 0, 0, 0); \
    ISSUE(X, 6, true); \
    const f32x4 S = S0 + S1; \
    *reinterpret_cast<f32x4*>(lw + L_S + (s_) * SREC + lane * 16) = S; \
    m = fmaxf(fmaxf(m, fmaxf(S[0], S[1])), fmaxf(S[2], S[3])); \
    asm volatile("" : "+v"(m)); \
    ISSUE(X, 7, true); \
    SB(); } while (0)
#pragma unroll 1
  for (int s = 0; s < NS; s += 2) {
    K_TILE(0, s);
    K_TILE(1, s + 1);
  }
  m = xrow_max(m);
  const float mc = m * QSCALE;
  const u32 lomask = c >= 8 ? 0xffff0000u : 0u;
  f32x4 acc[2][4], accz = {0.f, 0.f, 0.f, 0.f};
#pragma unroll
  for (int h = 0; h < 2; ++h)
#pragma unroll
    for (int e = 0; e < 4; ++e) acc[h][e] = f32x4{0.f, 0.f, 0.f, 0.f};
  float l = 0.f;
#define V_CVT(X, mp_, h_) do { _Pragma("unroll") for (int e = 0; e < 4; ++e) \
      vf[h_][e][mp_] = pk_bf16(i2f(R[8 * (X) + 4 * (mp_) + (h_)][e]), i2f(R[8 * (X) + 4 * (mp_) + (h_) + 2][e])); } while (0)
#define V_TILE(X, s_, ni_) do { \
    SLOT_ADDR(s_); \
    CLOCK_READ(); \
    const f32x4 S = *reinterpret_cast<const f32x4*>(lw + L_S + ((s_) - NS) * SREC + lane * 16); \
    const int vq = ((s_) - NS) * 16 + wave * 4 + g;                 \
    const f32x4 vsc = *reinterpret_cast<const f32x4*>(lds + L_VS + vq * 16); \
    const u32x2 vzn = *reinterpret_cast<const u32x2*>(lds + L_VZ + vq * 8); \
    ISSUE(X, 0, 0 < (ni_)); \
    float p[4]; u32 y[4]; \
    _Pragma("unroll") for (int r = 0; r < 4; ++r) p[r] = __builtin_amdgcn_exp2f(fmaf(S[r], QSCALE, -mc)); \
    l += (p[0] + p[1]) + (p[2] + p[3]); \
    asm volatile("" : "+v"(l)); \
    CLOCK_USE(s_); \
    ISSUE(X, 1, 1 < (ni_)); \
    _Pragma("unroll") for (int r = 0; r < 4; ++r) {     \
      const float x = p[r] * vsc[r]; \
      y[r] = __float_as_uint(x - __uint_as_float(__float_as_uint(x) & lomask)); } \
    u32x2 pfu = {__builtin_amdgcn_perm(y[1], y[0], 0x07060302u), __builtin_amdgcn_perm(y[3], y[2], 0x07060302u)}; \
    accz = __builtin_amdgcn_mfma_f32_16x16x16bf16_1k(__builtin_bit_cast(s16x4, vzn), __builtin_bit_cast(s16x4, pfu), accz, 0, 0, 0); \
    asm volatile("" : "+v"(pfu), "+v"(accz)); \
    const s16x4 pf = __builtin_bit_cast(s16x4, pfu); \
    u32x2 vf[2][4]; \
    ISSUE(X, 2, 2 < (ni_)); \
    V_CVT(X, 0, 0); ISSUE(X, 3, 3 < (ni_)); \
    V_CVT(X, 0, 1); ISSUE(X, 4, 4 < (ni_)); \
    V_CVT(X, 1, 0); ISSUE(X, 5, 5 < (ni_)); \
    V_CVT(X, 1, 1); ISSUE(X, 6, 6 < (ni_)); \
    _Pragma("unroll") for (int h = 0; h < 2; ++h) \
      _Pragma("unroll") for (int e = 0; e < 4; ++e) \
        acc[h][e] = __builtin_amdgcn_mfma_f32_16x16x16bf16_1k(__builtin_bit_cast(s16x4, vf[h][e]), pf, acc[h][e], 0, 0, 0); \
    ISSUE(X, 7, 7 < (ni_)); \
    SB(); } while (0)
#pragma unroll 1
  for (int s = NS; s < 2 * NS - 2; s += 2) {
    V_TILE(0, s, 8);
    V_TILE(1, s + 1, 8);
  }
  V_TILE(0, 2 * NS - 2, 16 - WIN);
  V_TILE(1, 2 * NS - 1, 0);
  STAMP(1);
  const rsrc_t rp = __builtin_amdgcn_make_buffer_rsrc((void*)(ws + (size_t)bh * WS_GRP), 0, WS_EPOCH + 8, 0x00020000);
  const int outw = ((bh * REP + (split >> 2)) * DH + ((split >> 1) & 1) * 64 + (split & 1) * 2) * 2;
  const u32 tagA = (ep0 + 1u) << 8, tagB = (ep1 + 1u) << 8;
  const int ib = (split * NSPLIT + 8 * wave) * PKT + lane * 16;
  u32x4 x = __builtin_amdgcn_raw_buffer_load_b128(rp, ib, 0, AUX_LD);
  SB();

  {
    l = xrow_sum(l);
    if (lane < 16) *reinterpret_cast<f32x4*>(lds + L_ML + (wave * 16 + lane) * 16) = f32x4{mc, l, accz[0], 0.f};
    unsigned char* orow = lw + L_S + c * OROW + g * 16;
#pragma unroll
    for (int h = 0; h < 2; ++h)
#pragma unroll
      for (int e = 0; e < 4; ++e) *reinterpret_cast<f32x4*>(orow + h * 256 + e * 64) = acc[h][e];
  }
  __syncthreads();
  STAMP(2);
  {
    const int h = tid >> 5, d4 = tid & 31;
    f32x4 ml[NW]; float zl[NW]; f32x4 a[NW], b[NW];
#pragma unroll
    for (int w = 0; w < NW; ++w) {
      ml[w] = *reinterpret_cast<const f32x4*>(lds + L_ML + (w * 16 + h) * 16);
      zl[w] = *reinterpret_cast<const float*>(lds + L_ML + (w * 16 + h + 8) * 16 + 8);
    }
#pragma unroll
    for (int w = 0; w < NW; ++w) {
      a[w] = *reinterpret_cast<const f32x4*>(lds + w * L_WAVE + L_S + h * OROW + d4 * 16);
      b[w] = *reinterpret_cast<const f32x4*>(lds + w * L_WAVE + L_S + (h + 8) * OROW + d4 * 16);
    }
    SB();
    const float M = fmaxf(fmaxf(ml[0][0], ml[1][0]), fmaxf(ml[2][0], ml[3][0]));
    float L = 0.f; f32x4 o = {0.f, 0.f, 0.f, 0.f};
#pragma unroll
    for (int w = 0; w < NW; ++w) {
      const float wt = __builtin_amdgcn_exp2f(ml[w][0] - M);
      L = fmaf(wt, ml[w][1], L);
      o += ((a[w] + ml[w][2]) + (b[w] + zl[w])) * wt;
    }
    const float mx = fmaxf(fmaxf(fabsf(o[0]), fabsf(o[1])), fmaxf(fabsf(o[2]), fabsf(o[3])));
    const int ex = max(__builtin_amdgcn_frexp_expf(mx), -110) - 15;
    const u32 mlb = (__float_as_uint((tid & 4) ? L : M) >> (8 * (tid & 3))) & 0xffu;
    const u32x4 gr = {pk_f16(__builtin_amdgcn_ldexpf(o[0], -ex), __builtin_amdgcn_ldexpf(o[1], -ex)), tagA | mlb,
                      pk_f16(__builtin_amdgcn_ldexpf(o[2], -ex), __builtin_amdgcn_ldexpf(o[3], -ex)), tagB | (u32)(ex + 128)};
    __builtin_amdgcn_raw_buffer_store_b128(gr, rp, ((tid >> 3) * NSPLIT + split) * PKT + (tid & 7) * 16, 0, AUX_ST);
    SB();
  }
  STAMP(3);
  {
    for (int spins = 0;; ++spins) {
      const bool need = (((x[1] ^ tagA) | (x[3] ^ tagB)) & 0xffffff00u) != 0u;
      const int nmiss = __builtin_popcountll(__builtin_amdgcn_ballot_w64(need));
      if (nmiss == 0 || spins > (1 << 20)) break;
      if (nmiss > 8 * NEAR && spins > 0) __builtin_amdgcn_s_sleep(SLEEP_FAR);
      asm volatile("" ::: "memory");
      const u32x4 t = __builtin_amdgcn_raw_buffer_load_b128(rp, need ? ib : OOB, 0, AUX_LD);
      x = need ? t : x;
    }
    STAMP(5);
    u32 mb = (x[1] & 0xffu) << (8 * (lane & 3));
    mb |= dppu<0xB1>(mb); mb |= dppu<0x4E>(mb);
    const u32 mo = dppu<0x141>(mb);
    const float pm = __uint_as_float((lane & 4) ? mo : mb), pl = __uint_as_float((lane & 4) ? mb : mo);
    const u32 x0 = x[0], x1 = x[2], xe = x[3];
    const float M = xrow_max(fmaxf(pm, dpp<0x128>(pm)));
    const float w = __builtin_amdgcn_exp2f(pm - M), wx = __builtin_amdgcn_ldexpf(w, (int)(xe & 0xffu) - 128);
    const f16x2 h0 = __builtin_bit_cast(f16x2, x0), h1 = __builtin_bit_cast(f16x2, x1);
    float v[5] = {(float)h0[0] * wx, (float)h0[1] * wx, (float)h1[0] * wx, (float)h1[1] * wx, w * pl};
#pragma unroll
    for (int k = 0; k < 5; ++k) v[k] = xrow_sum(v[k] + dpp<0x128>(v[k]));
    if (lane < 8) *reinterpret_cast<f32x4*>(lw + lane * 16) = f32x4{v[0], v[1], v[2], v[3]};
    if (lane == 8) *reinterpret_cast<f32x2*>(lw + 128) = f32x2{M, v[4]};
    __syncthreads();
    if (tid < 16) {
      f32x2 r[NW]; float s0[NW], s1[NW];
#pragma unroll
      for (int w = 0; w < NW; ++w) {
        r[w] = *reinterpret_cast<const f32x2*>(lds + w * L_WAVE + 128);
        s0[w] = *reinterpret_cast<const float*>(lds + w * L_WAVE + tid * 4);
        s1[w] = *reinterpret_cast<const float*>(lds + w * L_WAVE + tid * 4 + 64);
      }
      const float MM = fmaxf(fmaxf(r[0][0], r[1][0]), fmaxf(r[2][0], r[3][0]));
      float LL = 0.f, a0 = 0.f, a1 = 0.f;
#pragma unroll
      for (int w = 0; w < NW; ++w) { const float wt = __builtin_amdgcn_exp2f(r[w][0] - MM); LL = fmaf(wt, r[w][1], LL); a0 = fmaf(wt, s0[w], a0); a1 = fmaf(wt, s1[w], a1); }
      const float inv = __builtin_amdgcn_rcpf(LL);
      __builtin_amdgcn_raw_buffer_store_b32(pk_bf16(a0 * inv, a1 * inv), __builtin_amdgcn_make_buffer_rsrc((void*)out, 0, B_ * HQ * DH * 2, 0x00020000), tid * 8, outw, 0);
      if (split == 0 && tid == 0) __builtin_amdgcn_raw_buffer_store_b64(u32x2{ep0 + 1u, ep1 + 1u}, rp, 0, WS_EPOCH, AUX_SC1);
    }
  }
}

extern "C" void kernel_launch(void* const* d_in, const int* in_sizes, int n_in, void* d_out, int out_size,
                              void* d_ws, size_t ws_size, hipStream_t stream) {
  (void)hipFuncSetAttribute((const void*)kivi_decode, hipFuncAttributeMaxDynamicSharedMemorySize, LDS_BYTES);
  kivi_decode<<<NBH * NSPLIT, NW * 64, LDS_BYTES, stream>>>((const u32*)d_in[0], (const int*)d_in[1], (const int*)d_in[2],
      (const u32*)d_in[3], (const u32*)d_in[4], (const u32*)d_in[5], (const u32*)d_in[6], (unsigned char*)d_ws, (u32*)d_out);
}
```

```cpp
#include <hip/hip_runtime.h>
#include <hip/hip_bf16.h>
#include <stdint.h>
#define TT 110

typedef unsigned u32;
typedef u32 u32x4 __attribute__((ext_vector_type(4)));
typedef u32 u32x2 __attribute__((ext_vector_type(2)));
typedef float f32x4 __attribute__((ext_vector_type(4)));
typedef float f32x2 __attribute__((ext_vector_type(2)));
typedef __bf16 bf16x8 __attribute__((ext_vector_type(8)));
typedef __bf16 bf16x2 __attribute__((ext_vector_type(2)));
typedef _Float16 f16x2 __attribute__((ext_vector_type(2)));
typedef short s16x4 __attribute__((ext_vector_type(4)));
typedef __amdgpu_buffer_rsrc_t rsrc_t;

constexpr int B_ = 2, HQ = 32, HKV = 4, SKV = 32768, DH = 128, GT = 128, NGK = SKV / GT;
constexpr int REP = HQ / HKV;
constexpr int NBH = B_ * HKV;
constexpr int NW = 4;
constexpr int NSPLIT = 32;
constexpr int NS = 16;
#define SH 3
#define SO 1
constexpr float QSCALE = 0.088388347648318440f * 1.4426950408889634f;

constexpr int KROW = 272, L_S = 16 * KROW, SREC = 1024, L_WAVE = L_S + NS * SREC;
constexpr int OROW = (64 + 4) * 4;
static_assert(16 * OROW <= NS * SREC, "merge rows");
constexpr int L_KS = NW * L_WAVE, L_KZ = L_KS + NS * 512, L_VS = L_KZ + NS * 512, L_VZ = L_VS + NS * 256;
constexpr int L_ML = L_VZ + NS * 128, L_Q = L_ML + NW * 16 * 16, L_END = L_Q + REP * KROW;
constexpr int LDS_BYTES = 116 * 1024;
static_assert(L_END <= LDS_BYTES, "LDS map");

constexpr int PKT = 128, WS_EPOCH = NSPLIT * NSPLIT * PKT, WS_GRP = 256 * 1024;
constexpr int OOB = 0x7fffffe0;
constexpr int AUX_NT = 2, AUX_SC1 = 16;
#define WIN 12
#define W0 5
#define W1 6
#define W2 7
#define W3 9
#define DL 40
#define T0 170
static_assert(WIN == 12, "the issue points below assume a look-ahead of 12 loads");
#define VMWAIT(n_) __builtin_amdgcn_s_waitcnt(0x0f70 | ((n_) & 15) | (((n_) >> 4) << 14))
#define CT 0
#define AUX_LD 16
#define AUX_ST 16
#define DRAIN1 0
#define SLEEP1 20
#define FIRST 4
#define SLEEP_FAR 2
#define NEAR 2
#define PEEK 0

#define STAMP(i)

__device__ __forceinline__ float bf_lo(u32 x) { return __builtin_bit_cast(float, x << 16); }
__device__ __forceinline__ float bf_hi(u32 x) { return __builtin_bit_cast(float, x & 0xffff0000u); }
__device__ __forceinline__ u32 pk_bf16(float a, float b) {
  bf16x2 r; r[0] = (__bf16)a; r[1] = (__bf16)b; return __builtin_bit_cast(u32, r);
}
__device__ __forceinline__ float i2f(u32 x) { return (float)(int)x; }
template <int CTRL> __device__ __forceinline__ u32 dppu(u32 x) { return (u32)__builtin_amdgcn_mov_dpp((int)x, CTRL, 0xf, 0xf, true); }
template <int CTRL> __device__ __forceinline__ float dpp(float x) {
  return __builtin_bit_cast(float, __builtin_amdgcn_mov_dpp(__builtin_bit_cast(int, x), CTRL, 0xf, 0xf, true));
}
__device__ __forceinline__ u32 pk_f16(float a, float b) {
  f16x2 r; r[0] = (_Float16)a; r[1] = (_Float16)b; return __builtin_bit_cast(u32, r);
}
__device__ __forceinline__ float xrow_max(float x) {
  auto s = __builtin_amdgcn_permlane16_swap(__float_as_uint(x), __float_as_uint(x), false, false);
  x = fmaxf(__uint_as_float(s[0]), __uint_as_float(s[1]));
  auto t = __builtin_amdgcn_permlane32_swap(__float_as_uint(x), __float_as_uint(x), false, false);
  return fmaxf(__uint_as_float(t[0]), __uint_as_float(t[1]));
}
__device__ __forceinline__ float xrow_sum(float x) {
  auto s = __builtin_amdgcn_permlane16_swap(__float_as_uint(x), __float_as_uint(x), false, false);
  x = __uint_as_float(s[0]) + __uint_as_float(s[1]);
  auto t = __builtin_amdgcn_permlane32_swap(__float_as_uint(x), __float_as_uint(x), false, false);
  return __uint_as_float(t[0]) + __uint_as_float(t[1]);
}
#define SB() __builtin_amdgcn_sched_barrier(0)

__global__ void __launch_bounds__(NW * 64, 2)
kivi_decode(const u32* q, const int* qk, const int* qv,
            const u32* ks, const u32* kz,
            const u32* vs, const u32* vz, unsigned char* ws, u32* out) {
  extern __shared__ __attribute__((aligned(16))) unsigned char lds[];
  STAMP(0);
  const u32 ctouch = 0;
  const int tid = threadIdx.x, lane = tid & 63;
  const int wave = __builtin_amdgcn_readfirstlane(tid >> 6);
  const int c = lane & 15, g = lane >> 4;
  const int side = blockIdx.x & 1, idx = blockIdx.x >> 3;
  const int bh = (blockIdx.x & 6) + (idx >> 4), rank = idx & 15, split = 2 * rank + side;
  const int rnd0 = SH * bh + SO * side;
  const int voff = g * 2048 + c * 16;
  unsigned char* lw = lds + wave * L_WAVE;
  u32x4 R[16];
  bf16x8 qf[4];
  u32 ep0, ep1, tstart;
  auto round_of = [&](int i) { return side * 16 + ((i + rnd0) & 15); };
  auto soff_of = [&](int s) { return (round_of(s) * 64 + rank * 4 + wave) * 8192; };
  const int ko = (round_of(tid >> 4) * 8 + (rank >> 1)) * 256 + (tid & 15) * 16;
  const int vo = (round_of((tid & 127) >> 3) * 16 + rank) * 128 + (tid & 7) * 16;
  const int so0 = soff_of(0);
  asm volatile("" : "+s"(q), "+s"(qk), "+s"(qv), "+s"(ks), "+s"(kz), "+s"(vs), "+s"(vz), "+s"(ws), "+s"(out) : "v"(ko), "v"(vo), "v"(voff), "s"(so0), "s"(bh));
  const char* kb = (const char*)qk + (size_t)bh * SKV * (DH * 4);
  const char* vb = (const char*)qv + (size_t)bh * SKV * (DH * 4);
  auto rsrc_of = [&](int s) { return __builtin_amdgcn_make_buffer_rsrc((void*)(s >= NS ? vb : kb), 0, SKV * DH * 4, 0x00020000); };
#define LOAD_BIG(dst_, rs_, so_, n_) dst_ = __builtin_amdgcn_raw_buffer_load_b128(rs_, voff + ((n_) >> 1) * 512 + ((n_) & 1) * 256, so_, AUX_NT)
  {
    const rsrc_t rk = __builtin_amdgcn_make_buffer_rsrc((void*)kb, 0, SKV * DH * 4, 0x00020000);
    SB();
#pragma unroll
    for (int n = 0; n < FIRST; ++n) LOAD_BIG(R[n], rk, so0, n);
    SB();
    unsigned long long ts = __builtin_amdgcn_s_memrealtime();
    const u32x4 ksr = __builtin_amdgcn_raw_buffer_load_b128(__builtin_amdgcn_make_buffer_rsrc((void*)(ks + bh * (SKV / 2)), 0, SKV * 2, 0x00020000), ko, 0, 0);
    const u32x4 kzr = __builtin_amdgcn_raw_buffer_load_b128(__builtin_amdgcn_make_buffer_rsrc((void*)(kz + bh * (SKV / 2)), 0, SKV * 2, 0x00020000), ko, 0, 0);
    u32x4 vr = __builtin_amdgcn_raw_buffer_load_b128(__builtin_amdgcn_make_buffer_rsrc((void*)((wave < 2 ? vs : vz) + bh * (SKV / 2)), 0, SKV * 2, 0x00020000), vo, 0, 0);
    const u32x2 qr = __builtin_amdgcn_raw_buffer_load_b64(__builtin_amdgcn_make_buffer_rsrc((void*)q, 0, B_ * HQ * DH * 2, 0x00020000), tid * 8, bh * (REP * DH * 2), 0);
    SB();
#pragma unroll
    for (int n = FIRST; n < 8; ++n) LOAD_BIG(R[n], rk, so0, n);
    SB();
    typedef const __attribute__((address_space(4))) u32 cu32;
    cu32* wsc = (cu32*)(ws + (size_t)bh * WS_GRP);
    ep0 = wsc[WS_EPOCH / 4]; ep1 = wsc[WS_EPOCH / 4 + 1];
    u32 tch[8];
#pragma unroll
    for (int i = 0; i < 8; ++i) tch[i] = wsc[(split * NSPLIT + 8 * wave + i) * (PKT / 4)];
    SB();
#pragma unroll
    for (int n = 0; n < WIN - 8; ++n) LOAD_BIG(R[8 + n], rk, soff_of(1), n);
    SB();
#define UNPK(x_, i_) f32x4{bf_lo(x_[2 * (i_)]), bf_hi(x_[2 * (i_)]), bf_lo(x_[2 * (i_) + 1]), bf_hi(x_[2 * (i_) + 1])}
#pragma unroll
    for (int i = 0; i < 2; ++i) {
      *reinterpret_cast<f32x4*>(lds + L_KS + tid * 32 + i * 16) = UNPK(ksr, i);
      *reinterpret_cast<f32x4*>(lds + L_KZ + tid * 32 + i * 16) = -UNPK(kzr, i) * UNPK(ksr, i);
    }
    asm volatile("" : "+v"(vr));
    if (wave < 2) {
#pragma unroll
      for (int i = 0; i < 2; ++i) *reinterpret_cast<f32x4*>(lds + L_VS + tid * 32 + i * 16) = UNPK(vr, i);
    } else *reinterpret_cast<u32x4*>(lds + L_VZ + (tid & 127) * 16) = vr ^ 0x80008000u;
    *reinterpret_cast<u32x2*>(lds + L_Q + (tid >> 5) * KROW + (tid & 31) * 8) = qr;
    asm volatile("" : "+s"(ep0), "+s"(ep1), "+s"(ts) : "s"(tch[0]), "s"(tch[1]), "s"(tch[2]), "s"(tch[3]), "s"(tch[4]), "s"(tch[5]), "s"(tch[6]), "s"(tch[7]), "v"(ctouch));
    tstart = (u32)ts;
    asm volatile("s_waitcnt lgkmcnt(0)" ::: "memory");
    __builtin_amdgcn_s_barrier();
    asm volatile("" ::: "memory");
#pragma unroll
    for (int i = 0; i < 4; ++i) qf[i] = __builtin_bit_cast(bf16x8, *reinterpret_cast<const u32x4*>(lds + L_Q + (c & 7) * KROW + i * 64 + g * 16));
  }
  SB();

  unsigned char* kt_w = lw + g * (4 * KROW) + c * 8;
  const unsigned char* kt_r = lw + c * KROW + g * 16;
  float m = -INFINITY;
  int late = 0;
#define REFILL(X, n_) do { if ((n_) + WIN < 16 + 0 * (X)) LOAD_BIG(R[8 * (1 - (X)) + (n_) + WIN - 8], rs1, so1, (n_) + WIN - 8); \
                           else LOAD_BIG(R[8 * (X) + (n_) + WIN - 16], rs2, so2, (n_) + WIN - 16); } while (0)
#define SLOT_ADDR(s_) const rsrc_t rs1 = rsrc_of((s_) + 1), rs2 = rsrc_of((s_) + 2); const int so1 = soff_of((s_) + 1), so2 = soff_of((s_) + 2); \
                      (void)rs1; (void)so1; (void)rs2; (void)so2
#define ISSUE(X, k_, on_) do { SB(); if (on_) { VMWAIT(W3 - 1); \
      asm volatile("s_cmp_gt_i32 %0, %4\n\ts_cbranch_scc1 1f\n\ts_waitcnt vmcnt(%1)\n\t" \
                   "s_cmp_gt_i32 %0, %5\n\ts_cbranch_scc1 1f\n\ts_waitcnt vmcnt(%2)\n\t" \
                   "s_cmp_gt_i32 %0, 0\n\ts_cbranch_scc1 1f\n\ts_waitcnt vmcnt(%3)\n1:" \
                   :: "s"(late), "i"(W2 - 1), "i"(W1 - 1), "i"(W0 - 1), "i"(2 * DL), "i"(DL) : "scc"); \
      REFILL(X, k_); } SB(); } while (0)
#define CLOCK_READ() unsigned long long now = __builtin_amdgcn_s_memrealtime()
#define CLOCK_USE(s_) do { asm volatile("" : "+s"(now)); late = (int)((u32)now - tstart) - (T0 + TT * (s_)); } while (0)
#define K_CVT(X, n_) do { const int h = (n_) & 1; const u32x4 k4 = R[8 * (X) + (n_)]; \
      const u32x2 w = {pk_bf16(fmaf(i2f(k4[0]), sc[h][0], zc[h][0]), fmaf(i2f(k4[1]), sc[h][1], zc[h][1])), \
                       pk_bf16(fmaf(i2f(k4[2]), sc[h][2], zc[h][2]), fmaf(i2f(k4[3]), sc[h][3], zc[h][3]))}; \
      *reinterpret_cast<u32x2*>(kt_w + ((n_) >> 1) * KROW + h * 128) = w; } while (0)
#define K_TILE(X, s_) do { \
    SLOT_ADDR(s_); \
    CLOCK_READ(); \
    f32x4 sc[2], zc[2]; \
    _Pragma("unroll") for (int h = 0; h < 2; ++h) { \
      sc[h] = *reinterpret_cast<const f32x4*>(lds + L_KS + (s_) * 512 + h * 256 + c * 16); \
      zc[h] = *reinterpret_cast<const f32x4*>(lds + L_KZ + (s_) * 512 + h * 256 + c * 16); } \
    ISSUE(X, 0, true); \
    K_CVT(X, 0); K_CVT(X, 1); CLOCK_USE(s_); ISSUE(X, 1, true); \
    K_CVT(X, 2); K_CVT(X, 3); ISSUE(X, 2, true); \
    K_CVT(X, 4); K_CVT(X, 5); ISSUE(X, 3, true); \
    K_CVT(X, 6); K_CVT(X, 7); ISSUE(X, 4, true); \
    bf16x8 kf[4]; \
    _Pragma("unroll") for (int i = 0; i < 4; ++i) kf[i] = __builtin_bit_cast(bf16x8, *reinterpret_cast<const u32x4*>(kt_r + 64 * i)); \
    ISSUE(X, 5, true); \
    f32x4 S0 = {0.f, 0.f, 0.f, 0.f}, S1 = {0.f, 0.f, 0.f, 0.f}; \
    S0 = __builtin_amdgcn_mfma_f32_16x16x32_bf16(kf[0], qf[0], S0, 0, 0, 0); \
    S1 = __builtin_amdgcn_mfma_f32_16x16x32_bf16(kf[1], qf[1], S1, 0, 0, 0); \
    S0 = __builtin_amdgcn_mfma_f32_16x16x32_bf16(kf[2], qf[2], S0, 0, 0, 0); \
    S1 = __builtin_amdgcn_mfma_f32_16x16x32_bf16(kf[3], qf[3], S1, 0, 0, 0); \
    ISSUE(X, 6, true); \
    const f32x4 S = S0 + S1; \
    *reinterpret_cast<f32x4*>(lw + L_S + (s_) * SREC + lane * 16) = S; \
    m = fmaxf(fmaxf(m, fmaxf(S[0], S[1])), fmaxf(S[2], S[3])); \
    asm volatile("" : "+v"(m)); \
    ISSUE(X, 7, true); \
    SB(); } while (0)
#pragma unroll 1
  for (int s = 0; s < NS; s += 2) {
    K_TILE(0, s);
    K_TILE(1, s + 1);
  }
  m = xrow_max(m);
  if (lane < 8) *reinterpret_cast<float*>(lds + L_ML + lane * 16 + wave * 4) = m;
  asm volatile("s_waitcnt lgkmcnt(0)" ::: "memory");
  __builtin_amdgcn_s_barrier();
  asm volatile("" ::: "memory");
  const f32x4 m4 = *reinterpret_cast<const f32x4*>(lds + L_ML + (c & 7) * 16);
  const float mc = fmaxf(fmaxf(m4[0], m4[1]), fmaxf(m4[2], m4[3])) * QSCALE;
  const u32 mlo = c < 8 ? 0xffffffffu : 0u;
  f32x4 acc[4], accz = {0.f, 0.f, 0.f, 0.f};
#pragma unroll
  for (int e = 0; e < 4; ++e) acc[e] = f32x4{0.f, 0.f, 0.f, 0.f};
  float l = 0.f;
#define V_CVT(X, mp_, h_) do { _Pragma("unroll") for (int e = 0; e < 4; ++e) \
      vf[h_][e][mp_] = pk_bf16(i2f(R[8 * (X) + 4 * (mp_) + (h_)][e]), i2f(R[8 * (X) + 4 * (mp_) + (h_) + 2][e])); } while (0)
#define V_TILE(X, s_, ni_) do { \
    SLOT_ADDR(s_); \
    CLOCK_READ(); \
    const f32x4 S = *reinterpret_cast<const f32x4*>(lw + L_S + ((s_) - NS) * SREC + lane * 16); \
    const int vq = ((s_) - NS) * 16 + wave * 4 + g;                 \
    const f32x4 vsc = *reinterpret_cast<const f32x4*>(lds + L_VS + vq * 16); \
    const u32x2 vzn = *reinterpret_cast<const u32x2*>(lds + L_VZ + vq * 8); \
    ISSUE(X, 0, 0 < (ni_)); \
    float p[4]; \
    _Pragma("unroll") for (int r = 0; r < 4; ++r) p[r] = __builtin_amdgcn_exp2f(fmaf(S[r], QSCALE, -mc)); \
    l += (p[0] + p[1]) + (p[2] + p[3]); \
    asm volatile("" : "+v"(l)); \
    CLOCK_USE(s_); \
    ISSUE(X, 1, 1 < (ni_)); \
    const u32x2 pfu = {pk_bf16(p[0] * vsc[0], p[1] * vsc[1]), pk_bf16(p[2] * vsc[2], p[3] * vsc[3])}; \
    accz = __builtin_amdgcn_mfma_f32_16x16x16bf16_1k(__builtin_bit_cast(s16x4, vzn), __builtin_bit_cast(s16x4, pfu), accz, 0, 0, 0); \
    u32x2 pf[2] = {pfu & mlo, pfu & ~mlo};              \
    asm volatile("" : "+v"(pf[0]), "+v"(pf[1]), "+v"(accz)); \
    if ((ni_) == 0) {                                   \
      _Pragma("unroll") for (int e = 0; e < 4; ++e) acc[e] += accz; \
      l = xrow_sum(l); \
      if (lane < 8) *reinterpret_cast<float*>(lds + L_ML + 128 + lane * 16 + wave * 4) = l; \
      asm volatile("" : "+v"(acc[0]), "+v"(acc[1]), "+v"(acc[2]), "+v"(acc[3])); } \
    u32x2 vf[2][4]; \
    ISSUE(X, 2, 2 < (ni_)); \
    V_CVT(X, 0, 0); ISSUE(X, 3, 3 < (ni_)); \
    V_CVT(X, 0, 1); ISSUE(X, 4, 4 < (ni_)); \
    V_CVT(X, 1, 0); \
    _Pragma("unroll") for (int e = 0; e < 4; ++e) \
      acc[e] = __builtin_amdgcn_mfma_f32_16x16x16bf16_1k(__builtin_bit_cast(s16x4, vf[0][e]), __builtin_bit_cast(s16x4, pf[0]), acc[e], 0, 0, 0); \
    ISSUE(X, 5, 5 < (ni_)); \
    V_CVT(X, 1, 1); ISSUE(X, 6, 6 < (ni_)); \
    _Pragma("unroll") for (int e = 0; e < 4; ++e) \
      acc[e] = __builtin_amdgcn_mfma_f32_16x16x16bf16_1k(__builtin_bit_cast(s16x4, vf[1][e]), __builtin_bit_cast(s16x4, pf[1]), acc[e], 0, 0, 0); \
    ISSUE(X, 7, 7 < (ni_)); \
    SB(); } while (0)
#pragma unroll 1
  for (int s = NS; s < 2 * NS - 2; s += 2) {
    V_TILE(0, s, 8);
    V_TILE(1, s + 1, 8);
  }
  V_TILE(0, 2 * NS - 2, 16 - WIN);
  V_TILE(1, 2 * NS - 1, 0);
  STAMP(1);
  const rsrc_t rp = __builtin_amdgcn_make_buffer_rsrc((void*)(ws + (size_t)bh * WS_GRP), 0, WS_EPOCH + 8, 0x00020000);
  const int outw = ((bh * REP + (split >> 2)) * DH + ((split >> 1) & 1) * 64 + (split & 1) * 2) * 2;
  const u32 tagA = (ep0 + 1u) << 8, tagB = (ep1 + 1u) << 8;
  const int ib = (split * NSPLIT + 8 * wave) * PKT + lane * 16;
  u32x4 x = {0u, ~tagA, 0u, 0u};
  SB();

#pragma unroll
  for (int e = 0; e < 4; ++e) *reinterpret_cast<f32x4*>(lw + L_S + c * OROW + e * 64 + g * 16) = acc[e];
  __syncthreads();
  STAMP(2);
  {
    const int h = tid >> 5;
    f32x4 a[NW];
#pragma unroll
    for (int w = 0; w < NW; ++w) a[w] = *reinterpret_cast<const f32x4*>(lds + w * L_WAVE + L_S + (h + ((tid >> 4) & 1) * 8) * OROW + (tid & 15) * 16);
    const f32x4 ml = *reinterpret_cast<const f32x4*>(lds + L_ML + (tid & 4) * 32 + h * 16);
    SB();
    const f32x4 o = (a[0] + a[1]) + (a[2] + a[3]);
    const float M = fmaxf(fmaxf(ml[0], ml[1]), fmaxf(ml[2], ml[3])) * QSCALE, L = (ml[0] + ml[1]) + (ml[2] + ml[3]);
    const float mx = fmaxf(fmaxf(fabsf(o[0]), fabsf(o[1])), fmaxf(fabsf(o[2]), fabsf(o[3])));
    const int ex = max(__builtin_amdgcn_frexp_expf(mx), -110) - 15;
    const u32 mlb = (__float_as_uint((tid & 4) ? L : M) >> (8 * (tid & 3))) & 0xffu;
    const u32x4 gr = {pk_f16(__builtin_amdgcn_ldexpf(o[0], -ex), __builtin_amdgcn_ldexpf(o[1], -ex)), tagA | mlb,
                      pk_f16(__builtin_amdgcn_ldexpf(o[2], -ex), __builtin_amdgcn_ldexpf(o[3], -ex)), tagB | (u32)(ex + 128)};
    __builtin_amdgcn_raw_buffer_store_b128(gr, rp, ((tid >> 3) * NSPLIT + split) * PKT + (tid & 7) * 16, 0, AUX_ST);
    SB();
  }
  STAMP(3);
  {
    for (int spins = 0;; ++spins) {
      const bool need = (((x[1] ^ tagA) | (x[3] ^ tagB)) & 0xffffff00u) != 0u;
      const int nmiss = __builtin_popcountll(__builtin_amdgcn_ballot_w64(need));
      if (nmiss == 0 || spins > (1 << 20)) break;
      if (nmiss > 8 * NEAR && spins > 0) __builtin_amdgcn_s_sleep(SLEEP_FAR);
      if (spins == 0) __builtin_amdgcn_s_sleep(SLEEP1);
      asm volatile("" ::: "memory");
      const u32x4 t = __builtin_amdgcn_raw_buffer_load_b128(rp, need ? ib : OOB, 0, AUX_LD);
      x = need ? t : x;
    }
    STAMP(5);
    u32 mb = (x[1] & 0xffu) << (8 * (lane & 3));
    mb |= dppu<0xB1>(mb); mb |= dppu<0x4E>(mb);
    const u32 mo = dppu<0x141>(mb);
    const float pm = __uint_as_float((lane & 4) ? mo : mb), pl = __uint_as_float((lane & 4) ? mb : mo);
    const u32 x0 = x[0], x1 = x[2], xe = x[3];
    const float M = xrow_max(fmaxf(pm, dpp<0x128>(pm)));
    const float w = __builtin_amdgcn_exp2f(pm - M), wx = __builtin_amdgcn_ldexpf(w, (int)(xe & 0xffu) - 128);
    const f16x2 h0 = __builtin_bit_cast(f16x2, x0), h1 = __builtin_bit_cast(f16x2, x1);
    float v[5] = {(float)h0[0] * wx, (float)h0[1] * wx, (float)h1[0] * wx, (float)h1[1] * wx, w * pl};
#pragma unroll
    for (int k = 0; k < 5; ++k) v[k] = xrow_sum(v[k] + dpp<0x128>(v[k]));
    if (lane < 8) *reinterpret_cast<f32x4*>(lw + lane * 16) = f32x4{v[0], v[1], v[2], v[3]};
    if (lane == 8) *reinterpret_cast<f32x2*>(lw + 128) = f32x2{M, v[4]};
    __syncthreads();
    if (tid < 16) {
      f32x2 r[NW]; float s0[NW], s1[NW];
#pragma unroll
      for (int w = 0; w < NW; ++w) {
        r[w] = *reinterpret_cast<const f32x2*>(lds + w * L_WAVE + 128);
        s0[w] = *reinterpret_cast<const float*>(lds + w * L_WAVE + tid * 4);
        s1[w] = *reinterpret_cast<const float*>(lds + w * L_WAVE + tid * 4 + 64);
      }
      const float MM = fmaxf(fmaxf(r[0][0], r[1][0]), fmaxf(r[2][0], r[3][0]));
      float LL = 0.f, a0 = 0.f, a1 = 0.f;
#pragma unroll
      for (int w = 0; w < NW; ++w) { const float wt = __builtin_amdgcn_exp2f(r[w][0] - MM); LL = fmaf(wt, r[w][1], LL); a0 = fmaf(wt, s0[w], a0); a1 = fmaf(wt, s1[w], a1); }
      const float inv = __builtin_amdgcn_rcpf(LL);
      __builtin_amdgcn_raw_buffer_store_b32(pk_bf16(a0 * inv, a1 * inv), __builtin_amdgcn_make_buffer_rsrc((void*)out, 0, B_ * HQ * DH * 2, 0x00020000), tid * 8, outw, 0);
      if (split == 0 && tid == 0) __builtin_amdgcn_raw_buffer_store_b64(u32x2{ep0 + 1u, ep1 + 1u}, rp, 0, WS_EPOCH, AUX_SC1);
    }
  }
}

extern "C" void kernel_launch(void* const* d_in, const int* in_sizes, int n_in, void* d_out, int out_size,
                              void* d_ws, size_t ws_size, hipStream_t stream) {
  (void)hipFuncSetAttribute((const void*)kivi_decode, hipFuncAttributeMaxDynamicSharedMemorySize, LDS_BYTES);
  kivi_decode<<<NBH * NSPLIT, NW * 64, LDS_BYTES, stream>>>((const u32*)d_in[0], (const int*)d_in[1], (const int*)d_in[2],
      (const u32*)d_in[3], (const u32*)d_in[4], (const u32*)d_in[5], (const u32*)d_in[6], (unsigned char*)d_ws, (u32*)d_out);
}
```

```cpp
#include <hip/hip_runtime.h>
#include <hip/hip_bf16.h>
#include <stdint.h>
#define PROBE 0
#define AMP 10

typedef unsigned u32;
typedef u32 u32x4 __attribute__((ext_vector_type(4)));
typedef u32 u32x2 __attribute__((ext_vector_type(2)));
typedef float f32x4 __attribute__((ext_vector_type(4)));
typedef float f32x2 __attribute__((ext_vector_type(2)));
typedef __bf16 bf16x8 __attribute__((ext_vector_type(8)));
typedef __bf16 bf16x2 __attribute__((ext_vector_type(2)));
typedef _Float16 f16x2 __attribute__((ext_vector_type(2)));
typedef short s16x4 __attribute__((ext_vector_type(4)));
typedef __amdgpu_buffer_rsrc_t rsrc_t;

constexpr int B_ = 2, HQ = 32, HKV = 4, SKV = 32768, DH = 128, GT = 128, NGK = SKV / GT;
constexpr int REP = HQ / HKV;
constexpr int NBH = B_ * HKV;
constexpr int NW = 4;
constexpr int NSPLIT = 32;
constexpr int NS = 16;
#define SH 3
#define SO 1
constexpr float QSCALE = 0.088388347648318440f * 1.4426950408889634f;

constexpr int KROW = 272, L_S = 16 * KROW, SREC = 1024, L_WAVE = L_S + NS * SREC;
constexpr int OROW = (64 + 4) * 4;
static_assert(16 * OROW <= NS * SREC, "merge rows");
constexpr int L_KS = NW * L_WAVE, L_KZ = L_KS + NS * 512, L_VS = L_KZ + NS * 512, L_VZ = L_VS + NS * 256;
constexpr int L_ML = L_VZ + NS * 128, L_Q = L_ML + NW * 16 * 16, L_END = L_Q + REP * KROW;
constexpr int LDS_BYTES = 116 * 1024;
static_assert(L_END <= LDS_BYTES, "LDS map");

constexpr int PKT = 128, WS_EPOCH = NSPLIT * NSPLIT * PKT, WS_GRP = 256 * 1024;
constexpr int OOB = 0x7fffffe0;
constexpr int AUX_NT = 2, AUX_SC1 = 16;
#define WIN 12
#define W0 5
#define W1 6
#define W2 7
#define W3 9
#define DL 40
#define T0 170
#define TT 109
static_assert(WIN == 12, "the issue points below assume a look-ahead of 12 loads");
#define VMWAIT(n_) __builtin_amdgcn_s_waitcnt(0x0f70 | ((n_) & 15) | (((n_) >> 4) << 14))
#define CT 0
#define AUX_LD 16
#define AUX_ST 16
#define DRAIN1 0
#define SLEEP1 20
#define FIRST 4
#define SLEEP_FAR 2
#define NEAR 2
#define PEEK 0

#define STAMP(i)

__device__ __forceinline__ float bf_lo(u32 x) { return __builtin_bit_cast(float, x << 16); }
__device__ __forceinline__ float bf_hi(u32 x) { return __builtin_bit_cast(float, x & 0xffff0000u); }
__device__ __forceinline__ u32 pk_bf16(float a, float b) {
  bf16x2 r; r[0] = (__bf16)a; r[1] = (__bf16)b; return __builtin_bit_cast(u32, r);
}
__device__ __forceinline__ float i2f(u32 x) { return (float)(int)x; }
template <int CTRL> __device__ __forceinline__ u32 dppu(u32 x) { return (u32)__builtin_amdgcn_mov_dpp((int)x, CTRL, 0xf, 0xf, true); }
template <int CTRL> __device__ __forceinline__ float dpp(float x) {
  return __builtin_bit_cast(float, __builtin_amdgcn_mov_dpp(__builtin_bit_cast(int, x), CTRL, 0xf, 0xf, true));
}
__device__ __forceinline__ u32 pk_f16(float a, float b) {
  f16x2 r; r[0] = (_Float16)a; r[1] = (_Float16)b; return __builtin_bit_cast(u32, r);
}
__device__ __forceinline__ float xrow_max(float x) {
  auto s = __builtin_amdgcn_permlane16_swap(__float_as_uint(x), __float_as_uint(x), false, false);
  x = fmaxf(__uint_as_float(s[0]), __uint_as_float(s[1]));
  auto t = __builtin_amdgcn_permlane32_swap(__float_as_uint(x), __float_as_uint(x), false, false);
  return fmaxf(__uint_as_float(t[0]), __uint_as_float(t[1]));
}
__device__ __forceinline__ float xrow_sum(float x) {
  auto s = __builtin_amdgcn_permlane16_swap(__float_as_uint(x), __float_as_uint(x), false, false);
  x = __uint_as_float(s[0]) + __uint_as_float(s[1]);
  auto t = __builtin_amdgcn_permlane32_swap(__float_as_uint(x), __float_as_uint(x), false, false);
  return __uint_as_float(t[0]) + __uint_as_float(t[1]);
}
#define SB() __builtin_amdgcn_sched_barrier(0)

__global__ void __launch_bounds__(NW * 64, 2)
kivi_decode(const u32* q, const int* qk, const int* qv,
            const u32* ks, const u32* kz,
            const u32* vs, const u32* vz, unsigned char* ws, u32* out) {
  extern __shared__ __attribute__((aligned(16))) unsigned char lds[];
  unsigned long long tzero, tx = 0;
#define RT(t_) do { __builtin_amdgcn_sched_barrier(0); asm volatile("s_memrealtime %0\n\ts_waitcnt lgkmcnt(0)" : "=s"(t_) :: "memory"); __builtin_amdgcn_sched_barrier(0); } while (0)
  asm volatile("s_memrealtime %0" : "=s"(tzero) :: "memory");
  __builtin_amdgcn_sched_barrier(0);
  const u32 ctouch = 0;
  const int tid = threadIdx.x, lane = tid & 63;
  const int wave = __builtin_amdgcn_readfirstlane(tid >> 6);
  const int c = lane & 15, g = lane >> 4;
  const int side = blockIdx.x & 1, idx = blockIdx.x >> 3;
  const int bh = (blockIdx.x & 6) + (idx >> 4), rank = idx & 15, split = 2 * rank + side;
  const int rnd0 = SH * bh + SO * side;
  const int voff = g * 2048 + c * 16;
  unsigned char* lw = lds + wave * L_WAVE;
  u32x4 R[16];
  bf16x8 qf[4];
  u32 ep0, ep1, tstart;
  auto round_of = [&](int i) { return side * 16 + ((i + rnd0) & 15); };
  auto soff_of = [&](int s) { return (round_of(s) * 64 + rank * 4 + wave) * 8192; };
  const int ko = (round_of(tid >> 4) * 8 + (rank >> 1)) * 256 + (tid & 15) * 16;
  const int vo = (round_of((tid & 127) >> 3) * 16 + rank) * 128 + (tid & 7) * 16;
  const int so0 = soff_of(0);
  asm volatile("" : "+s"(q), "+s"(qk), "+s"(qv), "+s"(ks), "+s"(kz), "+s"(vs), "+s"(vz), "+s"(ws), "+s"(out) : "v"(ko), "v"(vo), "v"(voff), "s"(so0), "s"(bh));
  const char* kb = (const char*)qk + (size_t)bh * SKV * (DH * 4);
  const char* vb = (const char*)qv + (size_t)bh * SKV * (DH * 4);
  auto rsrc_of = [&](int s) { return __builtin_amdgcn_make_buffer_rsrc((void*)(s >= NS ? vb : kb), 0, SKV * DH * 4, 0x00020000); };
#define LOAD_BIG(dst_, rs_, so_, n_) dst_ = __builtin_amdgcn_raw_buffer_load_b128(rs_, voff + ((n_) >> 1) * 512 + ((n_) & 1) * 256, so_, AUX_NT)
  {
    const rsrc_t rk = __builtin_amdgcn_make_buffer_rsrc((void*)kb, 0, SKV * DH * 4, 0x00020000);
    SB();
#pragma unroll
    for (int n = 0; n < FIRST; ++n) LOAD_BIG(R[n], rk, so0, n);
    SB();
    unsigned long long ts = __builtin_amdgcn_s_memrealtime();
    const u32x4 ksr = __builtin_amdgcn_raw_buffer_load_b128(__builtin_amdgcn_make_buffer_rsrc((void*)(ks + bh * (SKV / 2)), 0, SKV * 2, 0x00020000), ko, 0, 0);
    const u32x4 kzr = __builtin_amdgcn_raw_buffer_load_b128(__builtin_amdgcn_make_buffer_rsrc((void*)(kz + bh * (SKV / 2)), 0, SKV * 2, 0x00020000), ko, 0, 0);
    u32x4 vr = __builtin_amdgcn_raw_buffer_load_b128(__builtin_amdgcn_make_buffer_rsrc((void*)((wave < 2 ? vs : vz) + bh * (SKV / 2)), 0, SKV * 2, 0x00020000), vo, 0, 0);
    const u32x2 qr = __builtin_amdgcn_raw_buffer_load_b64(__builtin_amdgcn_make_buffer_rsrc((void*)q, 0, B_ * HQ * DH * 2, 0x00020000), tid * 8, bh * (REP * DH * 2), 0);
    SB();
#pragma unroll
    for (int n = FIRST; n < 8; ++n) LOAD_BIG(R[n], rk, so0, n);
    SB();
    typedef const __attribute__((address_space(4))) u32 cu32;
    cu32* wsc = (cu32*)(ws + (size_t)bh * WS_GRP);
    ep0 = wsc[WS_EPOCH / 4]; ep1 = wsc[WS_EPOCH / 4 + 1];
    u32 tch[8];
#pragma unroll
    for (int i = 0; i < 8; ++i) tch[i] = wsc[(split * NSPLIT + 8 * wave + i) * (PKT / 4)];
    SB();
#pragma unroll
    for (int n = 0; n < WIN - 8; ++n) LOAD_BIG(R[8 + n], rk, soff_of(1), n);
    SB();
#define UNPK(x_, i_) f32x4{bf_lo(x_[2 * (i_)]), bf_hi(x_[2 * (i_)]), bf_lo(x_[2 * (i_) + 1]), bf_hi(x_[2 * (i_) + 1])}
#pragma unroll
    for (int i = 0; i < 2; ++i) {
      *reinterpret_cast<f32x4*>(lds + L_KS + tid * 32 + i * 16) = UNPK(ksr, i);
      *reinterpret_cast<f32x4*>(lds + L_KZ + tid * 32 + i * 16) = -UNPK(kzr, i) * UNPK(ksr, i);
    }
    asm volatile("" : "+v"(vr));
    if (wave < 2) {
#pragma unroll
      for (int i = 0; i < 2; ++i) *reinterpret_cast<f32x4*>(lds + L_VS + tid * 32 + i * 16) = UNPK(vr, i);
    } else *reinterpret_cast<u32x4*>(lds + L_VZ + (tid & 127) * 16) = vr ^ 0x80008000u;
    *reinterpret_cast<u32x2*>(lds + L_Q + (tid >> 5) * KROW + (tid & 31) * 8) = qr;
    asm volatile("" : "+s"(ep0), "+s"(ep1), "+s"(ts) : "s"(tch[0]), "s"(tch[1]), "s"(tch[2]), "s"(tch[3]), "s"(tch[4]), "s"(tch[5]), "s"(tch[6]), "s"(tch[7]), "v"(ctouch));
    tstart = (u32)ts;
    asm volatile("s_waitcnt lgkmcnt(0)" ::: "memory");
    __builtin_amdgcn_s_barrier();
    asm volatile("" ::: "memory");
#pragma unroll
    for (int i = 0; i < 4; ++i) qf[i] = __builtin_bit_cast(bf16x8, *reinterpret_cast<const u32x4*>(lds + L_Q + (c & 7) * KROW + i * 64 + g * 16));
  }
  SB();

  unsigned char* kt_w = lw + g * (4 * KROW) + c * 8;
  const unsigned char* kt_r = lw + c * KROW + g * 16;
  float m = -INFINITY;
  int late = 0;
#define REFILL(X, n_) do { if ((n_) + WIN < 16 + 0 * (X)) LOAD_BIG(R[8 * (1 - (X)) + (n_) + WIN - 8], rs1, so1, (n_) + WIN - 8); \
                           else LOAD_BIG(R[8 * (X) + (n_) + WIN - 16], rs2, so2, (n_) + WIN - 16); } while (0)
#define SLOT_ADDR(s_) const rsrc_t rs1 = rsrc_of((s_) + 1), rs2 = rsrc_of((s_) + 2); const int so1 = soff_of((s_) + 1), so2 = soff_of((s_) + 2); \
                      (void)rs1; (void)so1; (void)rs2; (void)so2
#define ISSUE(X, k_, on_) do { SB(); if (on_) { VMWAIT(W3 - 1); \
      asm volatile("s_cmp_gt_i32 %0, %4\n\ts_cbranch_scc1 1f\n\ts_waitcnt vmcnt(%1)\n\t" \
                   "s_cmp_gt_i32 %0, %5\n\ts_cbranch_scc1 1f\n\ts_waitcnt vmcnt(%2)\n\t" \
                   "s_cmp_gt_i32 %0, 0\n\ts_cbranch_scc1 1f\n\ts_waitcnt vmcnt(%3)\n1:" \
                   :: "s"(late), "i"(W2 - 1), "i"(W1 - 1), "i"(W0 - 1), "i"(2 * DL), "i"(DL) : "scc"); \
      REFILL(X, k_); } SB(); } while (0)
#define CLOCK_READ() unsigned long long now = __builtin_amdgcn_s_memrealtime()
#define CLOCK_USE(s_) do { asm volatile("" : "+s"(now)); late = (int)((u32)now - tstart) - (T0 + TT * (s_)); } while (0)
#define K_CVT(X, n_) do { const int h = (n_) & 1; const u32x4 k4 = R[8 * (X) + (n_)]; \
      const u32x2 w = {pk_bf16(fmaf(i2f(k4[0]), sc[h][0], zc[h][0]), fmaf(i2f(k4[1]), sc[h][1], zc[h][1])), \
                       pk_bf16(fmaf(i2f(k4[2]), sc[h][2], zc[h][2]), fmaf(i2f(k4[3]), sc[h][3], zc[h][3]))}; \
      *reinterpret_cast<u32x2*>(kt_w + ((n_) >> 1) * KROW + h * 128) = w; } while (0)
#define K_TILE(X, s_) do { \
    SLOT_ADDR(s_); \
    CLOCK_READ(); \
    f32x4 sc[2], zc[2]; \
    _Pragma("unroll") for (int h = 0; h < 2; ++h) { \
      sc[h] = *reinterpret_cast<const f32x4*>(lds + L_KS + (s_) * 512 + h * 256 + c * 16); \
      zc[h] = *reinterpret_cast<const f32x4*>(lds + L_KZ + (s_) * 512 + h * 256 + c * 16); } \
    ISSUE(X, 0, true); \
    K_CVT(X, 0); K_CVT(X, 1); CLOCK_USE(s_); ISSUE(X, 1, true); \
    K_CVT(X, 2); K_CVT(X, 3); ISSUE(X, 2, true); \
    K_CVT(X, 4); K_CVT(X, 5); ISSUE(X, 3, true); \
    K_CVT(X, 6); K_CVT(X, 7); ISSUE(X, 4, true); \
    bf16x8 kf[4]; \
    _Pragma("unroll") for (int i = 0; i < 4; ++i) kf[i] = __builtin_bit_cast(bf16x8, *reinterpret_cast<const u32x4*>(kt_r + 64 * i)); \
    ISSUE(X, 5, true); \
    f32x4 S0 = {0.f, 0.f, 0.f, 0.f}, S1 = {0.f, 0.f, 0.f, 0.f}; \
    S0 = __builtin_amdgcn_mfma_f32_16x16x32_bf16(kf[0], qf[0], S0, 0, 0, 0); \
    S1 = __builtin_amdgcn_mfma_f32_16x16x32_bf16(kf[1], qf[1], S1, 0, 0, 0); \
    S0 = __builtin_amdgcn_mfma_f32_16x16x32_bf16(kf[2], qf[2], S0, 0, 0, 0); \
    S1 = __builtin_amdgcn_mfma_f32_16x16x32_bf16(kf[3], qf[3], S1, 0, 0, 0); \
    ISSUE(X, 6, true); \
    const f32x4 S = S0 + S1; \
    *reinterpret_cast<f32x4*>(lw + L_S + (s_) * SREC + lane * 16) = S; \
    m = fmaxf(fmaxf(m, fmaxf(S[0], S[1])), fmaxf(S[2], S[3])); \
    asm volatile("" : "+v"(m)); \
    ISSUE(X, 7, true); \
    SB(); } while (0)
#pragma unroll 1
  for (int s = 0; s < NS; s += 2) {
    K_TILE(0, s);
    K_TILE(1, s + 1);
  }
  m = xrow_max(m);
  if (lane < 8) *reinterpret_cast<float*>(lds + L_ML + lane * 16 + wave * 4) = m;
  asm volatile("s_waitcnt lgkmcnt(0)" ::: "memory");
  __builtin_amdgcn_s_barrier();
  asm volatile("" ::: "memory");
  const f32x4 m4 = *reinterpret_cast<const f32x4*>(lds + L_ML + (c & 7) * 16);
  const float mc = fmaxf(fmaxf(m4[0], m4[1]), fmaxf(m4[2], m4[3])) * QSCALE;
  const u32 mlo = c < 8 ? 0xffffffffu : 0u;
  f32x4 acc[4], accz = {0.f, 0.f, 0.f, 0.f};
#pragma unroll
  for (int e = 0; e < 4; ++e) acc[e] = f32x4{0.f, 0.f, 0.f, 0.f};
  float l = 0.f;
#define V_CVT(X, mp_, h_) do { _Pragma("unroll") for (int e = 0; e < 4; ++e) \
      vf[h_][e][mp_] = pk_bf16(i2f(R[8 * (X) + 4 * (mp_) + (h_)][e]), i2f(R[8 * (X) + 4 * (mp_) + (h_) + 2][e])); } while (0)
#define V_TILE(X, s_, ni_) do { \
    SLOT_ADDR(s_); \
    CLOCK_READ(); \
    const f32x4 S = *reinterpret_cast<const f32x4*>(lw + L_S + ((s_) - NS) * SREC + lane * 16); \
    const int vq = ((s_) - NS) * 16 + wave * 4 + g;                 \
    const f32x4 vsc = *reinterpret_cast<const f32x4*>(lds + L_VS + vq * 16); \
    const u32x2 vzn = *reinterpret_cast<const u32x2*>(lds + L_VZ + vq * 8); \
    ISSUE(X, 0, 0 < (ni_)); \
    float p[4]; \
    _Pragma("unroll") for (int r = 0; r < 4; ++r) p[r] = __builtin_amdgcn_exp2f(fmaf(S[r], QSCALE, -mc)); \
    l += (p[0] + p[1]) + (p[2] + p[3]); \
    asm volatile("" : "+v"(l)); \
    CLOCK_USE(s_); \
    ISSUE(X, 1, 1 < (ni_)); \
    const u32x2 pfu = {pk_bf16(p[0] * vsc[0], p[1] * vsc[1]), pk_bf16(p[2] * vsc[2], p[3] * vsc[3])}; \
    accz = __builtin_amdgcn_mfma_f32_16x16x16bf16_1k(__builtin_bit_cast(s16x4, vzn), __builtin_bit_cast(s16x4, pfu), accz, 0, 0, 0); \
    u32x2 pf[2] = {pfu & mlo, pfu & ~mlo};              \
    asm volatile("" : "+v"(pf[0]), "+v"(pf[1]), "+v"(accz)); \
    if ((ni_) == 0) {                                   \
      _Pragma("unroll") for (int e = 0; e < 4; ++e) acc[e] += accz; \
      l = xrow_sum(l); \
      if (lane < 8) *reinterpret_cast<float*>(lds + L_ML + 128 + lane * 16 + wave * 4) = l; \
      asm volatile("" : "+v"(acc[0]), "+v"(acc[1]), "+v"(acc[2]), "+v"(acc[3])); } \
    u32x2 vf[2][4]; \
    ISSUE(X, 2, 2 < (ni_)); \
    V_CVT(X, 0, 0); ISSUE(X, 3, 3 < (ni_)); \
    V_CVT(X, 0, 1); ISSUE(X, 4, 4 < (ni_)); \
    V_CVT(X, 1, 0); \
    _Pragma("unroll") for (int e = 0; e < 4; ++e) \
      acc[e] = __builtin_amdgcn_mfma_f32_16x16x16bf16_1k(__builtin_bit_cast(s16x4, vf[0][e]), __builtin_bit_cast(s16x4, pf[0]), acc[e], 0, 0, 0); \
    ISSUE(X, 5, 5 < (ni_)); \
    V_CVT(X, 1, 1); ISSUE(X, 6, 6 < (ni_)); \
    _Pragma("unroll") for (int e = 0; e < 4; ++e) \
      acc[e] = __builtin_amdgcn_mfma_f32_16x16x16bf16_1k(__builtin_bit_cast(s16x4, vf[1][e]), __builtin_bit_cast(s16x4, pf[1]), acc[e], 0, 0, 0); \
    ISSUE(X, 7, 7 < (ni_)); \
    SB(); } while (0)
#pragma unroll 1
  for (int s = NS; s < 2 * NS - 2; s += 2) {
    V_TILE(0, s, 8);
    V_TILE(1, s + 1, 8);
  }
  V_TILE(0, 2 * NS - 2, 16 - WIN);
  V_TILE(1, 2 * NS - 1, 0);
  const rsrc_t rp = __builtin_amdgcn_make_buffer_rsrc((void*)(ws + (size_t)bh * WS_GRP), 0, WS_EPOCH + 8, 0x00020000);
  const int outw = ((bh * REP + (split >> 2)) * DH + ((split >> 1) & 1) * 64 + (split & 1) * 2) * 2;
  const u32 tagA = (ep0 + 1u) << 8, tagB = (ep1 + 1u) << 8;
  const int ib = (split * NSPLIT + 8 * wave) * PKT + lane * 16;
  u32x4 x = {0u, ~tagA, 0u, 0u};
  SB();

#pragma unroll
  for (int e = 0; e < 4; ++e) *reinterpret_cast<f32x4*>(lw + L_S + c * OROW + e * 64 + g * 16) = acc[e];
  __syncthreads();
  STAMP(2);
  {
    const int h = tid >> 5;
    f32x4 a[NW];
#pragma unroll
    for (int w = 0; w < NW; ++w) a[w] = *reinterpret_cast<const f32x4*>(lds + w * L_WAVE + L_S + (h + ((tid >> 4) & 1) * 8) * OROW + (tid & 15) * 16);
    const f32x4 ml = *reinterpret_cast<const f32x4*>(lds + L_ML + (tid & 4) * 32 + h * 16);
    SB();
    const f32x4 o = (a[0] + a[1]) + (a[2] + a[3]);
    const float M = fmaxf(fmaxf(ml[0], ml[1]), fmaxf(ml[2], ml[3])) * QSCALE, L = (ml[0] + ml[1]) + (ml[2] + ml[3]);
    const float mx = fmaxf(fmaxf(fabsf(o[0]), fabsf(o[1])), fmaxf(fabsf(o[2]), fabsf(o[3])));
    const int ex = max(__builtin_amdgcn_frexp_expf(mx), -110) - 15;
    const u32 mlb = (__float_as_uint((tid & 4) ? L : M) >> (8 * (tid & 3))) & 0xffu;
    const u32x4 gr = {pk_f16(__builtin_amdgcn_ldexpf(o[0], -ex), __builtin_amdgcn_ldexpf(o[1], -ex)), tagA | mlb,
                      pk_f16(__builtin_amdgcn_ldexpf(o[2], -ex), __builtin_amdgcn_ldexpf(o[3], -ex)), tagB | (u32)(ex + 128)};
    __builtin_amdgcn_raw_buffer_store_b128(gr, rp, ((tid >> 3) * NSPLIT + split) * PKT + (tid & 7) * 16, 0, AUX_ST);
    SB();
  }
  STAMP(3);
  {
    for (int spins = 0;; ++spins) {
      const bool need = (((x[1] ^ tagA) | (x[3] ^ tagB)) & 0xffffff00u) != 0u;
      const int nmiss = __builtin_popcountll(__builtin_amdgcn_ballot_w64(need));
      if (nmiss == 0 || spins > (1 << 20)) break;
      if (nmiss > 8 * NEAR && spins > 0) __builtin_amdgcn_s_sleep(SLEEP_FAR);
      if (spins == 0) __builtin_amdgcn_s_sleep(SLEEP1);
      asm volatile("" ::: "memory");
      const u32x4 t = __builtin_amdgcn_raw_buffer_load_b128(rp, need ? ib : OOB, 0, AUX_LD);
      x = need ? t : x;
    }
    STAMP(5);
    u32 mb = (x[1] & 0xffu) << (8 * (lane & 3));
    mb |= dppu<0xB1>(mb); mb |= dppu<0x4E>(mb);
    const u32 mo = dppu<0x141>(mb);
    const float pm = __uint_as_float((lane & 4) ? mo : mb), pl = __uint_as_float((lane & 4) ? mb : mo);
    const u32 x0 = x[0], x1 = x[2], xe = x[3];
    const float M = xrow_max(fmaxf(pm, dpp<0x128>(pm)));
    const float w = __builtin_amdgcn_exp2f(pm - M), wx = __builtin_amdgcn_ldexpf(w, (int)(xe & 0xffu) - 128);
    const f16x2 h0 = __builtin_bit_cast(f16x2, x0), h1 = __builtin_bit_cast(f16x2, x1);
    float v[5] = {(float)h0[0] * wx, (float)h0[1] * wx, (float)h1[0] * wx, (float)h1[1] * wx, w * pl};
#pragma unroll
    for (int k = 0; k < 5; ++k) v[k] = xrow_sum(v[k] + dpp<0x128>(v[k]));
    if (lane < 8) *reinterpret_cast<f32x4*>(lw + lane * 16) = f32x4{v[0], v[1], v[2], v[3]};
    if (lane == 8) *reinterpret_cast<f32x2*>(lw + 128) = f32x2{M, v[4]};
    __syncthreads();
    if (tid < 16) {
      f32x2 r[NW]; float s0[NW], s1[NW];
#pragma unroll
      for (int w = 0; w < NW; ++w) {
        r[w] = *reinterpret_cast<const f32x2*>(lds + w * L_WAVE + 128);
        s0[w] = *reinterpret_cast<const float*>(lds + w * L_WAVE + tid * 4);
        s1[w] = *reinterpret_cast<const float*>(lds + w * L_WAVE + tid * 4 + 64);
      }
      const float MM = fmaxf(fmaxf(r[0][0], r[1][0]), fmaxf(r[2][0], r[3][0]));
      float LL = 0.f, a0 = 0.f, a1 = 0.f;
#pragma unroll
      for (int w = 0; w < NW; ++w) { const float wt = __builtin_amdgcn_exp2f(r[w][0] - MM); LL = fmaf(wt, r[w][1], LL); a0 = fmaf(wt, s0[w], a0); a1 = fmaf(wt, s1[w], a1); }
      const float inv = __builtin_amdgcn_rcpf(LL);
      __builtin_amdgcn_raw_buffer_store_b32(pk_bf16(a0 * inv, a1 * inv), __builtin_amdgcn_make_buffer_rsrc((void*)out, 0, B_ * HQ * DH * 2, 0x00020000), tid * 8, outw, 0);
      if (split == 0 && tid == 0) __builtin_amdgcn_raw_buffer_store_b64(u32x2{ep0 + 1u, ep1 + 1u}, rp, 0, WS_EPOCH, AUX_SC1);
    }
  }
  tx = tzero;
  { const unsigned lim = 10000u + AMP * ((unsigned)tx - (unsigned)tzero);
    while ((unsigned)__builtin_amdgcn_s_memrealtime() - (unsigned)tzero < lim) __builtin_amdgcn_s_sleep(1); }
}

extern "C" void kernel_launch(void* const* d_in, const int* in_sizes, int n_in, void* d_out, int out_size,
                              void* d_ws, size_t ws_size, hipStream_t stream) {
  (void)hipFuncSetAttribute((const void*)kivi_decode, hipFuncAttributeMaxDynamicSharedMemorySize, LDS_BYTES);
  kivi_decode<<<NBH * NSPLIT, NW * 64, LDS_BYTES, stream>>>((const u32*)d_in[0], (const int*)d_in[1], (const int*)d_in[2],
      (const u32*)d_in[3], (const u32*)d_in[4], (const u32*)d_in[5], (const u32*)d_in[6], (unsigned char*)d_ws, (u32*)d_out);
}
```

```cpp
#include <hip/hip_runtime.h>
#include <hip/hip_bf16.h>
#include <stdint.h>

typedef unsigned u32;
typedef u32 u32x4 __attribute__((ext_vector_type(4)));
typedef u32 u32x2 __attribute__((ext_vector_type(2)));
typedef float f32x4 __attribute__((ext_vector_type(4)));
typedef __bf16 bf16x8 __attribute__((ext_vector_type(8)));
typedef __bf16 bf16x2 __attribute__((ext_vector_type(2)));
typedef _Float16 f16x2 __attribute__((ext_vector_type(2)));
typedef short s16x4 __attribute__((ext_vector_type(4)));
typedef __amdgpu_buffer_rsrc_t rsrc_t;

constexpr int B_ = 2, HQ = 32, HKV = 4, SKV = 32768, DH = 128;
constexpr int REP = HQ / HKV;
constexpr int NBH = B_ * HKV;
constexpr int NW = 4;
constexpr int NSPLIT = 32;
constexpr int NS = 16;
constexpr float QSCALE = 0.088388347648318440f * 1.4426950408889634f;

constexpr int KROW = 272, L_S = 16 * KROW, SREC = 1024, L_WAVE = L_S + NS * SREC;
constexpr int OROW = (64 + 4) * 4;
constexpr int L_KS = NW * L_WAVE, L_KZ = L_KS + NS * 512, L_VS = L_KZ + NS * 512, L_VZ = L_VS + NS * 256;
constexpr int L_ML = L_VZ + NS * 128, L_Q = L_ML + 256, LDS_BYTES = L_Q + REP * KROW;
static_assert(16 * OROW <= NS * SREC && LDS_BYTES > 80 * 1024 && LDS_BYTES <= 160 * 1024, "LDS map");

constexpr int PKT = 128, WS_EPOCH = NSPLIT * NSPLIT * PKT, WS_GRP = 256 * 1024;
constexpr int OOB = 0x7fffffe0;
constexpr int AUX_NT = 2, AUX_SC1 = 16;

constexpr int T0 = 190, TK2 = 216, TV2 = 219, DL = 28;
constexpr int T_POLL = T0 + (TK2 + TV2) * NS / 2 - 22, SLEEP1 = 18;
constexpr int WIN = 12, W0 = 5;

__device__ __forceinline__ float bf_lo(u32 x) { return __builtin_bit_cast(float, x << 16); }
__device__ __forceinline__ float bf_hi(u32 x) { return __builtin_bit_cast(float, x & 0xffff0000u); }
__device__ __forceinline__ f32x4 bf4(u32 a, u32 b) { return f32x4{bf_lo(a), bf_hi(a), bf_lo(b), bf_hi(b)}; }
__device__ __forceinline__ u32 pk_bf16(float a, float b) { bf16x2 r; r[0] = (__bf16)a; r[1] = (__bf16)b; return __builtin_bit_cast(u32, r); }
__device__ __forceinline__ u32 pk_f16(float a, float b) { f16x2 r; r[0] = (_Float16)a; r[1] = (_Float16)b; return __builtin_bit_cast(u32, r); }
__device__ __forceinline__ float i2f(u32 x) { return (float)(int)x; }
__device__ __forceinline__ float max4(f32x4 x) { return fmaxf(fmaxf(x[0], x[1]), fmaxf(x[2], x[3])); }
template <int CTRL> __device__ __forceinline__ u32 dppu(u32 x) { return (u32)__builtin_amdgcn_mov_dpp((int)x, CTRL, 0xf, 0xf, true); }
template <int CTRL> __device__ __forceinline__ float dpp(float x) { return __uint_as_float(dppu<CTRL>(__float_as_uint(x))); }
constexpr int XOR1 = 0xB1, XOR2 = 0x4E, HALF_MIRROR = 0x141, ROR8 = 0x128, SHL2 = 0x102;
__device__ __forceinline__ float xrow_max(float x) {
  auto s = __builtin_amdgcn_permlane16_swap(__float_as_uint(x), __float_as_uint(x), false, false);
  x = fmaxf(__uint_as_float(s[0]), __uint_as_float(s[1]));
  auto t = __builtin_amdgcn_permlane32_swap(__float_as_uint(x), __float_as_uint(x), false, false);
  return fmaxf(__uint_as_float(t[0]), __uint_as_float(t[1]));
}
__device__ __forceinline__ float xrow_sum(float x) {
  auto s = __builtin_amdgcn_permlane16_swap(__float_as_uint(x), __float_as_uint(x), false, false);
  x = __uint_as_float(s[0]) + __uint_as_float(s[1]);
  auto t = __builtin_amdgcn_permlane32_swap(__float_as_uint(x), __float_as_uint(x), false, false);
  return __uint_as_float(t[0]) + __uint_as_float(t[1]);
}
__device__ __forceinline__ rsrc_t mk_rsrc(const void* p, int bytes) { return __builtin_amdgcn_make_buffer_rsrc((void*)p, 0, bytes, 0x00020000); }
#define SB() __builtin_amdgcn_sched_barrier(0)
#define VMWAIT(n_) __builtin_amdgcn_s_waitcnt(0x0f70 | (n_))
#define LDS_BARRIER() do { asm volatile("s_waitcnt lgkmcnt(0)" ::: "memory"); __builtin_amdgcn_s_barrier(); asm volatile("" ::: "memory"); } while (0)

__global__ void __launch_bounds__(NW * 64, 2)
kivi_decode(const u32* q, const int* qk, const int* qv, const u32* ks, const u32* kz, const u32* vs, const u32* vz,
            unsigned char* ws, u32* out) {
  extern __shared__ __attribute__((aligned(16))) unsigned char lds[];
  const int tid = threadIdx.x, lane = tid & 63;
  const int wave = __builtin_amdgcn_readfirstlane(tid >> 6);
  const int c = lane & 15, g = lane >> 4;
  const int side = blockIdx.x & 1, idx = blockIdx.x >> 3;
  const int bh = (blockIdx.x & 6) + (idx >> 4), rank = idx & 15, split = 2 * rank + side;
  const int rnd0 = 3 * bh + side;
  auto blk_of = [&](int i) { return (side * 16 + ((i + rnd0) & 15)) * 16 + rank; };
  auto soff_of = [&](int s) { return blk_of(s) * 32768 + wave * 8192; };
  const int voff = g * 2048 + c * 16;
  const int ko = (blk_of(tid >> 4) >> 1) * 256 + (tid & 15) * 16;
  const int vo = blk_of((tid & 127) >> 3) * 128 + (tid & 7) * 16;
  const int so0 = soff_of(0);
  asm volatile("" : "+s"(q), "+s"(qk), "+s"(qv), "+s"(ks), "+s"(kz), "+s"(vs), "+s"(vz), "+s"(ws), "+s"(out)
                  : "v"(ko), "v"(vo), "v"(voff), "s"(so0), "s"(bh));
  const char* kb = (const char*)qk + (size_t)bh * SKV * (DH * 4);
  const char* vb = (const char*)qv + (size_t)bh * SKV * (DH * 4);
  auto rsrc_of = [&](int s) { return mk_rsrc(s >= NS ? vb : kb, SKV * DH * 4); };
  unsigned char* lw = lds + wave * L_WAVE;

  u32x4 R[16];
  bf16x8 qf[4];
  u32 ep0, ep1, tstart;
#define LOAD_BIG(dst_, rs_, so_, n_) dst_ = __builtin_amdgcn_raw_buffer_load_b128(rs_, voff + ((n_) >> 1) * 512 + ((n_) & 1) * 256, so_, AUX_NT)
  {
    const rsrc_t rk = mk_rsrc(kb, SKV * DH * 4);
    SB();
#pragma unroll
    for (int n = 0; n < 2; ++n) LOAD_BIG(R[n], rk, so0, n);
    SB();
    unsigned long long ts = __builtin_amdgcn_s_memrealtime();
    const u32x4 ksr = __builtin_amdgcn_raw_buffer_load_b128(mk_rsrc(ks + bh * (SKV / 2), SKV * 2), ko, 0, 0);
    const u32x4 kzr = __builtin_amdgcn_raw_buffer_load_b128(mk_rsrc(kz + bh * (SKV / 2), SKV * 2), ko, 0, 0);
    u32x4 vr = __builtin_amdgcn_raw_buffer_load_b128(mk_rsrc((wave < 2 ? vs : vz) + bh * (SKV / 2), SKV * 2), vo, 0, 0);
    const u32x2 qr = __builtin_amdgcn_raw_buffer_load_b64(mk_rsrc(q, B_ * HQ * DH * 2), tid * 8, bh * (REP * DH * 2), 0);
    SB();
#pragma unroll
    for (int n = 2; n < 8; ++n) LOAD_BIG(R[n], rk, so0, n);
    SB();
    typedef const __attribute__((address_space(4))) u32 cu32;
    cu32* epp = (cu32*)(ws + (size_t)bh * WS_GRP + WS_EPOCH);
    ep0 = epp[0]; ep1 = epp[1];
    SB();
#pragma unroll
    for (int n = 0; n < WIN - 8; ++n) LOAD_BIG(R[8 + n], rk, soff_of(1), n);
    SB();
#pragma unroll
    for (int i = 0; i < 2; ++i) {
      const f32x4 s4 = bf4(ksr[2 * i], ksr[2 * i + 1]);
      *reinterpret_cast<f32x4*>(lds + L_KS + tid * 32 + i * 16) = s4;
      *reinterpret_cast<f32x4*>(lds + L_KZ + tid * 32 + i * 16) = -bf4(kzr[2 * i], kzr[2 * i + 1]) * s4;
    }
    asm volatile("" : "+v"(vr));
    if (wave < 2) {
#pragma unroll
      for (int i = 0; i < 2; ++i) *reinterpret_cast<f32x4*>(lds + L_VS + tid * 32 + i * 16) = bf4(vr[2 * i], vr[2 * i + 1]);
    } else *reinterpret_cast<u32x4*>(lds + L_VZ + (tid & 127) * 16) = vr ^ 0x80008000u;
    *reinterpret_cast<u32x2*>(lds + L_Q + (tid >> 5) * KROW + (tid & 31) * 8) = qr;
    asm volatile("" : "+s"(ep0), "+s"(ep1), "+s"(ts));
    tstart = (u32)ts;
    LDS_BARRIER();
#pragma unroll
    for (int i = 0; i < 4; ++i) qf[i] = __builtin_bit_cast(bf16x8, *reinterpret_cast<const u32x4*>(lds + L_Q + (c & 7) * KROW + i * 64 + g * 16));
  }
  SB();

  unsigned char* kt_w = lw + g * (4 * KROW) + c * 8;
  const unsigned char* kt_r = lw + c * KROW + g * 16;
  float m = -INFINITY;
  int late = 0;
#define SLOT_ADDR(s_) const rsrc_t rs1 = rsrc_of((s_) + 1), rs2 = rsrc_of((s_) + 2); const int so1 = soff_of((s_) + 1), so2 = soff_of((s_) + 2); \
                      (void)rs1; (void)so1; (void)rs2; (void)so2
#define ISSUE(X, k_, on_) do { SB(); if (on_) { VMWAIT(W0 + 2); \
      asm volatile("s_cmp_gt_i32 %0, %4\n\ts_cbranch_scc1 1f\n\ts_waitcnt vmcnt(%1)\n\t" \
                   "s_cmp_gt_i32 %0, %5\n\ts_cbranch_scc1 1f\n\ts_waitcnt vmcnt(%2)\n\t" \
                   "s_cmp_gt_i32 %0, 0\n\ts_cbranch_scc1 1f\n\ts_waitcnt vmcnt(%3)\n1:" \
                   :: "s"(late), "i"(W0 + 1), "i"(W0), "i"(W0 - 1), "i"(2 * DL), "i"(DL) : "scc"); \
      if ((k_) < 16 - WIN) LOAD_BIG(R[8 * (1 - (X)) + (k_) + WIN - 8], rs1, so1, (k_) + WIN - 8); \
      else LOAD_BIG(R[8 * (X) + (k_) + WIN - 16], rs2, so2, (k_) + WIN - 16); } SB(); } while (0)
#define CLOCK_READ() unsigned long long now = __builtin_amdgcn_s_memrealtime()
#define CLOCK_USE(s_) do { asm volatile("" : "+s"(now)); late = (int)((u32)now - tstart) - ((s_) < NS ? T0 + TK2 * (s_) / 2 : T0 + TK2 * NS / 2 + TV2 * ((s_) - NS) / 2) + ((s_) == NS - 1 ? 1000 : 0); } while (0)

#define K_CVT(X, n_) do { const int h = (n_) & 1; const u32x4 k4 = R[8 * (X) + (n_)]; \
      const u32x2 w = {pk_bf16(fmaf(i2f(k4[0]), sc[h][0], zc[h][0]), fmaf(i2f(k4[1]), sc[h][1], zc[h][1])), \
                       pk_bf16(fmaf(i2f(k4[2]), sc[h][2], zc[h][2]), fmaf(i2f(k4[3]), sc[h][3], zc[h][3]))}; \
      *reinterpret_cast<u32x2*>(kt_w + ((n_) >> 1) * KROW + h * 128) = w; } while (0)
#define K_TILE(X, s_) do { \
    SLOT_ADDR(s_); \
    CLOCK_READ(); \
    f32x4 sc[2], zc[2]; \
    _Pragma("unroll") for (int h = 0; h < 2; ++h) { \
      sc[h] = *reinterpret_cast<const f32x4*>(lds + L_KS + (s_) * 512 + h * 256 + c * 16); \
      zc[h] = *reinterpret_cast<const f32x4*>(lds + L_KZ + (s_) * 512 + h * 256 + c * 16); } \
    ISSUE(X, 0, true); \
    K_CVT(X, 0); K_CVT(X, 1); CLOCK_USE(s_); ISSUE(X, 1, true); \
    K_CVT(X, 2); K_CVT(X, 3); ISSUE(X, 2, true); \
    K_CVT(X, 4); K_CVT(X, 5); ISSUE(X, 3, true); \
    K_CVT(X, 6); K_CVT(X, 7); ISSUE(X, 4, true); \
    bf16x8 kf[4];                                       \
    asm volatile("" ::: "memory"); \
    _Pragma("unroll") for (int i = 0; i < 4; ++i) kf[i] = __builtin_bit_cast(bf16x8, *reinterpret_cast<const u32x4*>(kt_r + 64 * i)); \
    asm volatile("" ::: "memory"); \
    ISSUE(X, 5, true); \
    f32x4 S0 = {0.f, 0.f, 0.f, 0.f}, S1 = {0.f, 0.f, 0.f, 0.f}; \
    S0 = __builtin_amdgcn_mfma_f32_16x16x32_bf16(kf[0], qf[0], S0, 0, 0, 0); \
    S1 = __builtin_amdgcn_mfma_f32_16x16x32_bf16(kf[1], qf[1], S1, 0, 0, 0); \
    S0 = __builtin_amdgcn_mfma_f32_16x16x32_bf16(kf[2], qf[2], S0, 0, 0, 0); \
    S1 = __builtin_amdgcn_mfma_f32_16x16x32_bf16(kf[3], qf[3], S1, 0, 0, 0); \
    ISSUE(X, 6, true); \
    const f32x4 S = S0 + S1; \
    *reinterpret_cast<f32x4*>(lw + L_S + (s_) * SREC + lane * 16) = S; \
    m = fmaxf(m, max4(S)); \
    asm volatile("" : "+v"(m)); \
    ISSUE(X, 7, true); } while (0)
#pragma unroll 1
  for (int s = 0; s < NS; s += 2) {
    K_TILE(0, s);
    K_TILE(1, s + 1);
  }
  m = xrow_max(m);
  if (lane < 8) *reinterpret_cast<float*>(lds + L_ML + lane * 16 + wave * 4) = m;
  LDS_BARRIER();
  m = max4(*reinterpret_cast<const f32x4*>(lds + L_ML + (c & 7) * 16));
  const u32 mlo = c < 8 ? 0xffffffffu : 0u;
  f32x4 acc[4], accz = {0.f, 0.f, 0.f, 0.f};
#pragma unroll
  for (int e = 0; e < 4; ++e) acc[e] = f32x4{0.f, 0.f, 0.f, 0.f};
  float l = 0.f;

#define V_CVT(X, mp_, h_) do { _Pragma("unroll") for (int e = 0; e < 4; ++e) \
      vf[h_][e][mp_] = pk_bf16(i2f(R[8 * (X) + 4 * (mp_) + (h_)][e]), i2f(R[8 * (X) + 4 * (mp_) + (h_) + 2][e])); } while (0)
#define V_MMA(h_) do { _Pragma("unroll") for (int e = 0; e < 4; ++e) \
      acc[e] = __builtin_amdgcn_mfma_f32_16x16x16bf16_1k(__builtin_bit_cast(s16x4, vf[h_][e]), __builtin_bit_cast(s16x4, pf[h_]), acc[e], 0, 0, 0); } while (0)
#define V_TILE(X, s_, ni_) do {                         \
    SLOT_ADDR(s_); \
    CLOCK_READ(); \
    const f32x4 S = *reinterpret_cast<const f32x4*>(lw + L_S + ((s_) - NS) * SREC + lane * 16); \
    const int vq = ((s_) - NS) * 16 + wave * 4 + g;     \
    const f32x4 vsc = *reinterpret_cast<const f32x4*>(lds + L_VS + vq * 16); \
    const u32x2 vzn = *reinterpret_cast<const u32x2*>(lds + L_VZ + vq * 8); \
    ISSUE(X, 0, 0 < (ni_)); \
    float p[4]; \
    _Pragma("unroll") for (int r = 0; r < 4; ++r) p[r] = __builtin_amdgcn_exp2f((S[r] - m) * QSCALE); \
    l += (p[0] + p[1]) + (p[2] + p[3]); \
    asm volatile("" : "+v"(l)); \
    if (1 < (ni_)) CLOCK_USE(s_); \
    ISSUE(X, 1, 1 < (ni_)); \
    const u32x2 pfu = {pk_bf16(p[0] * vsc[0], p[1] * vsc[1]), pk_bf16(p[2] * vsc[2], p[3] * vsc[3])}; \
    accz = __builtin_amdgcn_mfma_f32_16x16x16bf16_1k(__builtin_bit_cast(s16x4, vzn), __builtin_bit_cast(s16x4, pfu), accz, 0, 0, 0); \
    u32x2 pf[2] = {pfu & mlo, pfu & ~mlo};              \
    asm volatile("" : "+v"(pf[0]), "+v"(pf[1]), "+v"(accz)); \
    if ((ni_) == 0) {                                   \
      _Pragma("unroll") for (int e = 0; e < 4; ++e) acc[e] += accz; \
      l = xrow_sum(l); \
      if (lane < 8) *reinterpret_cast<float*>(lds + L_ML + 128 + lane * 16 + wave * 4) = l; \
      asm volatile("" : "+v"(acc[0]), "+v"(acc[1]), "+v"(acc[2]), "+v"(acc[3])); } \
    u32x2 vf[2][4]; \
    ISSUE(X, 2, 2 < (ni_)); \
    V_CVT(X, 0, 0); ISSUE(X, 3, 3 < (ni_)); \
    V_CVT(X, 0, 1); ISSUE(X, 4, 4 < (ni_)); \
    V_CVT(X, 1, 0); V_MMA(0); ISSUE(X, 5, 5 < (ni_)); \
    V_CVT(X, 1, 1); ISSUE(X, 6, 6 < (ni_)); \
    V_MMA(1); ISSUE(X, 7, 7 < (ni_)); } while (0)
#pragma unroll 1
  for (int s = NS; s < 2 * NS - 2; s += 2) {
    V_TILE(0, s, 8);
    V_TILE(1, s + 1, 8);
  }
  V_TILE(0, 2 * NS - 2, 16 - WIN);
  const rsrc_t rp = mk_rsrc(ws + (size_t)bh * WS_GRP, WS_EPOCH + 8);
  const u32 warm = __builtin_amdgcn_raw_buffer_load_b32(rp, lane == 0 ? WS_EPOCH : OOB, 0, AUX_SC1);
  SB();
  V_TILE(1, 2 * NS - 1, 0);

#pragma unroll
  for (int e = 0; e < 4; ++e) *reinterpret_cast<f32x4*>(lw + L_S + c * OROW + e * 64 + g * 16) = acc[e];
  LDS_BARRIER();
  const u32 tagA = (ep0 + 1u) << 8, tagB = (ep1 + 1u) << 8;
  {
    const int h = tid >> 5;
    f32x4 a[NW];
#pragma unroll
    for (int w = 0; w < NW; ++w) a[w] = *reinterpret_cast<const f32x4*>(lds + w * L_WAVE + L_S + (h + ((tid >> 4) & 1) * 8) * OROW + ((tid & 6) * 2 + ((tid >> 2) & 2) + (tid & 1)) * 16);
    const f32x4 ml = *reinterpret_cast<const f32x4*>(lds + L_ML + (tid & 4) * 32 + h * 16);
    SB();
    const f32x4 o = (a[0] + a[1]) + (a[2] + a[3]);
    const float mlv = (tid & 4) ? (ml[0] + ml[1]) + (ml[2] + ml[3]) : max4(ml);
    const float mx = fmaxf(fmaxf(fabsf(o[0]), fabsf(o[1])), fmaxf(fabsf(o[2]), fabsf(o[3])));
    const int ex = max(__builtin_amdgcn_frexp_expf(mx), -110) - 15;
    const u32 mlb = (__float_as_uint(mlv) >> (8 * (tid & 3))) & 0xffu;
    const u32x4 gr = {pk_f16(__builtin_amdgcn_ldexpf(o[0], -ex), __builtin_amdgcn_ldexpf(o[1], -ex)), tagA | mlb,
                      pk_f16(__builtin_amdgcn_ldexpf(o[2], -ex), __builtin_amdgcn_ldexpf(o[3], -ex)), tagB | (u32)(ex + 128)};
    __builtin_amdgcn_raw_buffer_store_b128(gr, rp, ((tid >> 3) * NSPLIT + split) * PKT + (tid & 7) * 16, 0, AUX_SC1);
    SB();
  }
  asm volatile("" :: "v"(warm));
  if (wave == 0) {
    const int ib = split * NSPLIT * PKT + lane * 16;
    int oo = (c & 10) == 0 ? (bh * REP * DH + split * 32 + 16 * (c & 1) + 4 * ((g & 1) * 2 + (g >> 1)) + (c >> 1)) * 2 : OOB;
    asm volatile("" : "+v"(oo));
    __builtin_amdgcn_s_sleep(SLEEP1);
    while ((u32)__builtin_amdgcn_s_memrealtime() - tstart < (u32)T_POLL) __builtin_amdgcn_s_sleep(1);
    asm volatile("" ::: "memory");
    u32x4 x[4];
#pragma unroll
    for (int j = 0; j < 4; ++j) x[j] = __builtin_amdgcn_raw_buffer_load_b128(rp, ib + j * 8 * PKT, 0, AUX_SC1);
    for (int spins = 0;; ++spins) {
      u32 bad[4];
#pragma unroll
      for (int j = 0; j < 4; ++j) bad[j] = (x[j][1] ^ tagA) | (x[j][3] ^ tagB);
      if (__builtin_amdgcn_ballot_w64(((bad[0] | bad[1]) | (bad[2] | bad[3])) > 0xffu) == 0 || spins > (1 << 20)) break;
      __builtin_amdgcn_s_sleep(2);
      asm volatile("" ::: "memory");
      u32x4 t[4];
#pragma unroll
      for (int j = 0; j < 4; ++j) t[j] = __builtin_amdgcn_raw_buffer_load_b128(rp, bad[j] > 0xffu ? ib + j * 8 * PKT : OOB, 0, AUX_SC1);
      SB();
#pragma unroll
      for (int j = 0; j < 4; ++j) x[j] = bad[j] > 0xffu ? t[j] : x[j];
    }
    f32x4 pm, pl;
#pragma unroll
    for (int j = 0; j < 4; ++j) {
      u32 mb = (x[j][1] & 0xffu) << (8 * (lane & 3));
      mb |= dppu<XOR1>(mb); mb |= dppu<XOR2>(mb);
      const u32 mo = dppu<HALF_MIRROR>(mb);
      pm[j] = __uint_as_float((lane & 4) ? mo : mb); pl[j] = __uint_as_float((lane & 4) ? mb : mo);
    }
    float M = max4(pm);
    M = xrow_max(fmaxf(M, dpp<ROR8>(M)));
    float v[4] = {0.f, 0.f, 0.f, 0.f}, L = 0.f;
#pragma unroll
    for (int j = 0; j < 4; ++j) {
      const float w = __builtin_amdgcn_exp2f((pm[j] - M) * QSCALE), wx = __builtin_amdgcn_ldexpf(w, (int)(x[j][3] & 0xffu) - 128);
      const u32 x0 = x[j][0], x2 = x[j][2];
      const f16x2 h0 = __builtin_bit_cast(f16x2, x0), h1 = __builtin_bit_cast(f16x2, x2);
      v[0] = fmaf((float)h0[0], wx, v[0]); v[1] = fmaf((float)h0[1], wx, v[1]);
      v[2] = fmaf((float)h1[0], wx, v[2]); v[3] = fmaf((float)h1[1], wx, v[3]);
      L = fmaf(w, pl[j], L);
    }
    auto t0 = __builtin_amdgcn_permlane32_swap(__float_as_uint(v[0]), __float_as_uint(v[1]), false, false);
    auto t1 = __builtin_amdgcn_permlane32_swap(__float_as_uint(v[2]), __float_as_uint(v[3]), false, false);
    auto t2 = __builtin_amdgcn_permlane16_swap(__float_as_uint(__uint_as_float(t0[0]) + __uint_as_float(t0[1])),
                                               __float_as_uint(__uint_as_float(t1[0]) + __uint_as_float(t1[1])), false, false);
    float o = __uint_as_float(t2[0]) + __uint_as_float(t2[1]);
    o += dpp<ROR8>(o);
    L = xrow_sum(L + dpp<ROR8>(L));
    o *= __builtin_amdgcn_rcpf(L);
    __builtin_amdgcn_raw_buffer_store_b32(pk_bf16(o, dpp<SHL2>(o)), mk_rsrc(out, B_ * HQ * DH * 2), oo, 0, AUX_SC1);
    if (split == 0) __builtin_amdgcn_raw_buffer_store_b64(u32x2{ep0 + 1u, ep1 + 1u}, rp, lane == 0 ? WS_EPOCH : OOB, 0, AUX_SC1);
  }
}

extern "C" void kernel_launch(void* const* d_in, const int* in_sizes, int n_in, void* d_out, int out_size,
                              void* d_ws, size_t ws_size, hipStream_t stream) {
  (void)hipFuncSetAttribute((const void*)kivi_decode, hipFuncAttributeMaxDynamicSharedMemorySize, LDS_BYTES);
  kivi_decode<<<NBH * NSPLIT, NW * 64, LDS_BYTES, stream>>>((const u32*)d_in[0], (const int*)d_in[1], (const int*)d_in[2],
      (const u32*)d_in[3], (const u32*)d_in[4], (const u32*)d_in[5], (const u32*)d_in[6], (unsigned char*)d_ws, (u32*)d_out);
}
```
